```python
import torch

def _softmax(x, axis=-1):
    return torch.softmax(x, dim=axis)
def _gelu(x):
    return torch.nn.functional.gelu(x, approximate="tanh")
def _silu(x):
    return torch.nn.functional.silu(x)
def _sigmoid(x):
    return torch.sigmoid(x)
def _layernorm(x, axis=-1, eps=1e-5):
    m = x.mean(dim=axis, keepdim=True)
    v = x.var(dim=axis, keepdim=True, unbiased=False)
    return (x - m) / torch.sqrt(v + eps)
def _scatter_add(tgt, idx, src, axis=0):
    return tgt.clone().index_add_(axis, idx, src)
def _segment_sum(src, seg, n):
    out = torch.zeros((n,) + src.shape[1:], dtype=src.dtype)
    return out.index_add_(0, seg, src)
def _pad(x, pw):
    flat = tuple(v for pair in reversed(pw) for v in pair)
    return torch.nn.functional.pad(x, flat)

kernel_name = 'recurrent_scan__mha_block__mixer_channel_mlp__prompt'

def setup_inputs():

    _w = (lambda a: (lambda t: t.to(torch.bfloat16) if t.is_floating_point() else t)(torch.as_tensor(a)))
    def _A_setup():
        B, S, D, H = 1, 2048, 640, 32
        return {
            "x": _w((lambda *s: torch.randn(*s, dtype=torch.bfloat16) * 0.05)(B, S, D)),
            "wqkv": _w((lambda *s: torch.randn(*s, dtype=torch.bfloat16) * 0.05)(D, 3 * D)),
            "wo": _w((lambda *s: torch.randn(*s, dtype=torch.bfloat16) * 0.05)(D, D)),
        }
    _A_in = _A_setup()

    def _B_setup():
        B, S, D, F = 1, 2048, 640, 1728
        return {
            "x": _w((lambda *s: torch.randn(*s, dtype=torch.bfloat16) * 0.05)(B, S, D)),
            "w1": _w((lambda *s: torch.randn(*s, dtype=torch.bfloat16) * 3.2)(D, F) * (D ** -0.5)),
            "b1": _w((lambda *s: torch.randn(*s, dtype=torch.bfloat16) * 1.6)(F)),
            "w2": _w((lambda *s: torch.randn(*s, dtype=torch.bfloat16) * 0.05)(F, D) * (F ** -0.5)),
            "b2": _w((lambda *s: torch.randn(*s, dtype=torch.bfloat16) * 0.05)(D)),
        }
    _B_in = _B_setup()

    return {
        "A_x": _A_in["x"],
        "A_wqkv": _A_in["wqkv"],
        "A_wo": _A_in["wo"],
        "B_x": _B_in["x"],
        "B_w1": _B_in["w1"],
        "B_b1": _B_in["b1"],
        "B_w2": _B_in["w2"],
        "B_b2": _B_in["b2"],
    }

def reference(A_x, A_wqkv, A_wo, B_x, B_w1, B_b1, B_w2, B_b2):
    def _A_ref(x, wqkv, wo):
        B, S, D = x.shape
        H = 32
        Dh = D // H
        qkv = (lambda x, *d: x.permute(*d))((x @ wqkv).reshape(B, S, 3, H, Dh), 2, 0, 3, 1, 4)
        q, k, v = qkv[0], qkv[1], qkv[2]
        scores = torch.einsum("bhqd,bhkd->bhqk", q, k) * (Dh ** -0.5)
        p = _softmax(scores, axis=-1)
        o = torch.einsum("bhqk,bhkd->bhqd", p, v)
        return (lambda x, *d: x.permute(*d))(o, 0, 2, 1, 3).reshape(B, S, D) @ wo

    def _B_ref(x, w1, b1, w2, b2):
        y = _layernorm(x, axis=-1)
        h = _gelu(torch.einsum("bsd,df->bsf", y, w1) + b1)
        return torch.einsum("bsf,fd->bsd", h, w2) + b2

    x = A_x
    xn = _layernorm(x, axis=-1)
    s = _A_ref(xn, A_wqkv, A_wo)
    g = _silu(_B_ref(xn, B_w1, B_b1, B_w2, B_b2))
    return x + s * g
```

```cpp
#include <hip/hip_runtime.h>

typedef unsigned u32;
typedef unsigned long long u64;
typedef u32 u32x2 __attribute__((ext_vector_type(2)));
typedef u32 u32x4 __attribute__((ext_vector_type(4)));
typedef short s16x4 __attribute__((ext_vector_type(4)));
typedef __bf16 bf2 __attribute__((ext_vector_type(2)));
typedef __bf16 bf8 __attribute__((ext_vector_type(8)));
typedef float f2 __attribute__((ext_vector_type(2)));
typedef float f4 __attribute__((ext_vector_type(4)));
typedef float f16v __attribute__((ext_vector_type(16)));
typedef float f32v __attribute__((ext_vector_type(32)));
typedef __amdgpu_buffer_rsrc_t rsrc_t;
#define LDSP(T) __attribute__((address_space(3))) T*
#define GLP(T) __attribute__((address_space(1))) T*
#define DEV __device__ __forceinline__

constexpr u32 SLOT = 2048 * 16;
constexpr u32 WS_FLAG = 0;
constexpr u32 F_NONCE = 0, F_QKV = 1024, F_H = F_QKV + 2048, F_O = F_H + 2048;
constexpr u32 WS_QK = 65536, SZ_QK = 160 * SLOT;
constexpr u32 VHEAD = 64 * 1296;
constexpr u32 WS_V = WS_QK + SZ_QK, SZ_V = 32 * VHEAD;
constexpr u32 WS_H = WS_V + SZ_V, SZ_H = 216 * SLOT;
constexpr u32 WS_W = WS_H + SZ_H, SZ_W = 296 * 640 * 16;
constexpr u32 WS_O = WS_W + SZ_W, SZ_O = 80 * SLOT;
constexpr u32 OOB = 0x80000000u;
constexpr int SC1 = 16;
constexpr u32 SPIN_MAX = 1u << 20;

constexpr u32 L_STAT = 148480, L_CS = L_STAT + 4096, L_A1 = L_CS + 4096;
constexpr u32 L_K = 0, L_V = 65536, L_R = L_V + VHEAD;

DEV rsrc_t mk(const void* p, u32 bytes) { return __builtin_amdgcn_make_buffer_rsrc((void*)p, 0, bytes, 0x00020000); }
DEV void dma(rsrc_t rs, u32 voff, u32 soff, u32 lds) {
  asm volatile("s_mov_b32 m0, %0\n\ts_nop 0\n\tbuffer_load_dwordx4 %1, %2, %3 offen lds" ::"s"(lds), "v"(voff), "s"(rs), "s"(soff) : "m0", "memory");
}
DEV void dma_sc1(rsrc_t rs, u32 voff, u32 soff, u32 lds) {
  asm volatile("s_mov_b32 m0, %0\n\ts_nop 0\n\tbuffer_load_dwordx4 %1, %2, %3 offen sc1 lds" ::"s"(lds), "v"(voff), "s"(rs), "s"(soff) : "m0", "memory");
}
template <int N> DEV void wait_bar() { asm volatile("s_waitcnt vmcnt(%0) lgkmcnt(0)\n\ts_barrier" ::"i"(N) : "memory"); }
DEV void lds_bar() { asm volatile("s_waitcnt lgkmcnt(0)\n\ts_barrier" ::: "memory"); }
template <int N> DEV void wait_vm() { asm volatile("s_waitcnt vmcnt(%0)" ::"i"(N) : "memory"); }
template <u32 C> DEV u32 xorc(u32 a) { u32 r; asm volatile("v_xor_b32 %0, %1, %2" : "=v"(r) : "i"(C), "v"(a)); return r; }
DEV u32x4 lds128(u32 a) { return *(LDSP(u32x4))a; }
DEV f4 ldsf4(u32 a) { return *(LDSP(f4))a; }
DEV u32x4 trfrag(u32 a) {
  s16x4 lo = __builtin_amdgcn_ds_read_tr16_b64_v4i16((LDSP(s16x4))a);
  s16x4 hi = __builtin_amdgcn_ds_read_tr16_b64_v4i16((LDSP(s16x4))(a + 1024));
  u32x2 l = __builtin_bit_cast(u32x2, lo), h = __builtin_bit_cast(u32x2, hi);
  return u32x4{l[0], l[1], h[0], h[1]};
}
DEV f16v mfma32(u32x4 a, u32x4 b, f16v c) { return __builtin_amdgcn_mfma_f32_32x32x16_bf16(__builtin_bit_cast(bf8, a), __builtin_bit_cast(bf8, b), c, 0, 0, 0); }
DEV f16v mfma32k8(u32x2 a, u32x2 b, f16v c) { return __builtin_amdgcn_mfma_f32_32x32x8bf16_1k(__builtin_bit_cast(s16x4, a), __builtin_bit_cast(s16x4, b), c, 0, 0, 0); }
DEV f4 mfma16(u32x4 a, u32x4 b, f4 c) { return __builtin_amdgcn_mfma_f32_16x16x32_bf16(__builtin_bit_cast(bf8, a), __builtin_bit_cast(bf8, b), c, 0, 0, 0); }
DEV float dot2(u32 a, u32 b, float c) { return __builtin_amdgcn_fdot2_f32_bf16(__builtin_bit_cast(bf2, a), __builtin_bit_cast(bf2, b), c, false); }
DEV u32 pk(float a, float b) { return __builtin_bit_cast(u32, __builtin_convertvector(f2{a, b}, bf2)); }
DEV float bflo(u32 v) { return __builtin_bit_cast(float, v << 16); }
DEV float bfhi(u32 v) { return __builtin_bit_cast(float, v & 0xffff0000u); }
DEV u32 swap2(u32 c) { return ((c & 1) << 1) | (c >> 1); }
DEV u32 perm32(u32 r) { return (r & 19) | ((r & 4) << 1) | ((r & 8) >> 1); }
DEV u64 ldflag(const char* p) { return __hip_atomic_load((GLP(const u64))p, __ATOMIC_RELAXED, __HIP_MEMORY_SCOPE_AGENT); }
DEV void stflag(char* p, u64 v) { __hip_atomic_store((GLP(u64))p, v, __ATOMIC_RELAXED, __HIP_MEMORY_SCOPE_AGENT); }
DEV void poll(const char* p, u64 tag) {
  for (u32 i = 0; i < SPIN_MAX; i++) {
    if (__all(ldflag(p) == tag)) break;
    __builtin_amdgcn_s_sleep(2);
  }
  __builtin_amdgcn_fence(__ATOMIC_ACQUIRE, "wavefront");
}

template <int I, int N, class F> DEV void sfor(F&& f) {
  if constexpr (I < N) { f(std::integral_constant<int, I>{}); sfor<I + 1, N>(f); }
}

constexpr int NSLOT = 9;
constexpr int consumed(int s) { return s <= 10 ? 2 * s : 10 + s; }
constexpr int issued(int s) { return consumed(s) + NSLOT < 30 ? consumed(s) + NSLOT : 30; }
constexpr int PUB_S = 13;

struct P1Out { f16v acc[2]; float a2; };

template <bool ISV>
DEV void phase1(const void* x, const void* wqkv, const void* w1, const void* b1, char* ws, u64 tag,
                int w, u32 lane, int rb, int j, int rot, P1Out& o) {
  const rsrc_t rx = mk(x, 2048 * 1280), rq = mk(wqkv, 640 * 3840), r1 = mk(w1, 640 * 3456);
  const int mw = w & 3, nw = w >> 2;
  const u32 h = lane >> 5, l31 = lane & 31, pr = perm32(l31);
  const u32 r4 = lane >> 4;
  const u32 vx0 = (rb * 128 + 16 * w + (lane >> 3)) * 1280 + (((lane & 7) ^ r4) << 4);
  const u32 vx1 = (vx0 + 8 * 1280) ^ 64;
  const u32 wch = (lane & 15) ^ (r4 << 2);
  const u32 vq = (8 * w + r4) * 3840 + j * 256 + wch * 16;
  const u32 v1 = j * 128 + wch * 8 < 1728 ? (8 * w + r4) * 3456 + j * 256 + wch * 16 : OOB;
  const u32 ldw = w * 2048;
  auto issue = [&](auto U) {
    constexpr int u = U, st = u < 20 ? u >> 1 : u - 20;
    int kt = st + rot; kt = kt >= 10 ? kt - 10 : kt;
    const u32 dst = (u % NSLOT) * 16384 + ldw;
    if constexpr (u >= 20) { dma(r1, v1, kt * 64 * 3456, dst); dma(r1, v1, kt * 64 * 3456 + 4 * 3456, dst + 1024); }
    else if constexpr (u & 1) { dma(rq, vq, kt * 64 * 3840, dst); dma(rq, vq, kt * 64 * 3840 + 4 * 3840, dst + 1024); }
    else { dma(rx, vx0, kt * 128, dst); dma(rx, vx1, kt * 128, dst + 1024); }
  };
  sfor<0, NSLOT>(issue);

  const u32 rho = mw ^ (nw << 1);
  const u32 row = 32 * mw + pr;
  const u32 ax = row * 128 + (((2 * rho + h) ^ ((row >> 1) & 7)) << 4);
  const u32 q = (lane & 15) >> 2;
  const u32 aw = rho * 4096 + (8 * h + q) * 256 + (((2 * nw) ^ q) << 6) + ((lane >> 4) & 1) * 32 + swap2(lane & 3) * 8;
  u32 bias[2];
#pragma unroll
  for (int t = 0; t < 2; t++) bias[t] = __builtin_amdgcn_raw_buffer_load_b16(mk(b1, 3456), (j * 128 + nw * 64 + t * 32 + pr) * 2, 0, 0);

  u32x4 X[40], Wf[2][2];
  f16v acc[2] = {};
  float sx = 0, sq = 0, cs[2] = {0, 0};
  u32x2 xs;
  auto rd = [&](auto P) {
    constexpr int p = P, s = p >> 2, pos = p & 3;
    constexpr int uw = s < 10 ? 2 * s + 1 : 10 + s;
    if constexpr (s < 10) X[p] = lds128(xorc<((2 * s) % NSLOT) * 16384 + pos * 32>(ax));
    Wf[p & 1][0] = trfrag(xorc<(uw % NSLOT) * 16384 + pos * 4096>(aw));
    Wf[p & 1][1] = trfrag(xorc<(uw % NSLOT) * 16384 + pos * 4096 + 64>(aw));
  };
  auto correct = [&](bool swapped, bool useb) {
#pragma unroll
    for (int t = 0; t < 2; t++) {
      f4 c4 = ldsf4(L_CS + ((nw * 2 + t) * 64 + lane) * 16);
      float c = (c4[0] + c4[1]) + (c4[2] + c4[3]);
      u32 ch = pk(c, c) & 0xffff;
      u32x2 wsd = {pk(c, c - bflo(ch)), ch | (useb ? bias[t] << 16 : 0)};
      acc[t] = swapped ? mfma32k8(xs, wsd, acc[t]) : mfma32k8(wsd, xs, acc[t]);
    }
  };
  constexpr int NST = ISV ? 5 : 4;
  wait_bar<2 * (issued(0) - consumed(1)) + 2>();
  rd(std::integral_constant<int, 0>{});
  sfor<0, 80>([&](auto P) {
    constexpr int p = P, s = p >> 2, pos = p & 3;
    if constexpr (pos == 3) {
      if constexpr (s + 1 >= PUB_S) wait_bar<0>();
      else wait_bar<2 * (issued(s) - consumed(s + 2)) + (s >= 10 ? NST : 0)>();
      if constexpr (s + 1 == PUB_S) { if (threadIdx.x == 0) stflag(ws + F_QKV + (j * 16 + rb) * 8, tag); }
    }
    if constexpr (p + 1 < 80) rd(std::integral_constant<int, p + 1>{});
    if constexpr (pos == 3 && s < 19) sfor<issued(s), issued(s + 1)>(issue);
    const u32x4 xf = X[p % 40];
#pragma unroll
    for (int t = 0; t < 2; t++) acc[t] = (ISV && s < 10) ? mfma32(xf, Wf[p & 1][t], acc[t]) : mfma32(Wf[p & 1][t], xf, acc[t]);
    if constexpr (pos == 0) {
#pragma unroll
      for (int i = 0; i < 4; i++) { cs[0] = dot2(Wf[p & 1][0][i], 0x3f803f80u, cs[0]); cs[1] = dot2(Wf[p & 1][1][i], 0x3f803f80u, cs[1]); }
    }
    if constexpr (pos == 1 && s < 10) {
#pragma unroll
      for (int i = 0; i < 8; i++) { u32 v = X[p - 1 + (i >> 2)][i & 3]; sx = dot2(v, 0x3f803f80u, sx); sq = dot2(v, v, sq); }
    }
    if constexpr (pos == 1 && (s == 9 || s == 19)) {
      if constexpr (s == 9) {
        *(LDSP(float))(L_STAT + (mw * 32 + l31) * 16 + (nw * 2 + h) * 4) = sx;
        *(LDSP(float))(L_STAT + 2048 + (mw * 32 + l31) * 16 + (nw * 2 + h) * 4) = sq;
      }
#pragma unroll
      for (int t = 0; t < 2; t++) { *(LDSP(float))(L_CS + ((nw * 2 + t) * 64 + lane) * 16 + mw * 4) = cs[t]; cs[t] = 0; }
    }
    __builtin_amdgcn_sched_barrier(0);
    if constexpr (p == 39) {
      f4 s4 = ldsf4(L_STAT + (mw * 32 + l31) * 16), q4 = ldsf4(L_STAT + 2048 + (mw * 32 + l31) * 16);
      float mu = ((s4[0] + s4[1]) + (s4[2] + s4[3])) * (1.f / 640);
      float var = fmaxf(((q4[0] + q4[1]) + (q4[2] + q4[3])) * (1.f / 640) - mu * mu, 0.f);
      float a1 = __builtin_amdgcn_rsqf(var + 1e-5f);
      o.a2 = a1 * __builtin_amdgcn_rsqf(var * a1 * a1 + 1e-5f);
      float inv = __builtin_amdgcn_rcpf(o.a2);
      u32 mh = pk(-mu, -mu), ih = pk(inv, inv);
      xs = u32x2{mh, pk(-mu - bflo(mh), h ? inv - bflo(ih) : inv)};
      correct(ISV, false);
      if constexpr (ISV) {
        *(LDSP(float))(L_A1 + w * 128 + pr * 4) = a1;
        const rsrc_t rv = mk(ws + WS_V, SZ_V);
        const u32 tile = (rb * 4 + mw) * 1296;
        u32 vones = OOB;
#pragma unroll
        for (int t = 0; t < 2; t++) {
          const u32 cv = (j - 10) * 128 + nw * 64 + t * 32 + pr, hd = cv / 20, d = cv - hd * 20;
          if (d == 0 && h == t) vones = hd * VHEAD + 1280;
#pragma unroll
          for (int g = 0; g < 2; g++) {
            f4 sa = ldsf4(L_A1 + w * 128 + (16 * g + 8 * h) * 4), sb = ldsf4(L_A1 + w * 128 + (16 * g + 8 * h) * 4 + 16);
            const f16v& a = acc[t];
            u32x4 dv = {pk(a[8 * g] * sa[0], a[8 * g + 1] * sa[1]), pk(a[8 * g + 2] * sa[2], a[8 * g + 3] * sa[3]),
                        pk(a[8 * g + 4] * sb[0], a[8 * g + 5] * sb[1]), pk(a[8 * g + 6] * sb[2], a[8 * g + 7] * sb[3])};
            __builtin_amdgcn_raw_buffer_store_b128(dv, rv, hd * VHEAD + (h * 20 + d) * 16 + g * 640, tile, SC1);
          }
        }
        __builtin_amdgcn_raw_buffer_store_b128(u32x4{0x3f803f80u, 0x3f803f80u, 0x3f803f80u, 0x3f803f80u}, rv, vones, tile, SC1);
      } else {
        const rsrc_t rk = mk(ws + WS_QK, SZ_QK);
        const float sc = j < 5 ? a1 * (0.22360679774997896f * 1.4426950408889634f) : a1;
        const u32 vo = h * SLOT + (rb * 128 + row) * 16;
#pragma unroll
        for (int t = 0; t < 2; t++)
#pragma unroll
          for (int g = 0; g < 2; g++) {
            const f16v& a = acc[t];
            u32x4 dv = {pk(a[8 * g] * sc, a[8 * g + 1] * sc), pk(a[8 * g + 2] * sc, a[8 * g + 3] * sc),
                        pk(a[8 * g + 4] * sc, a[8 * g + 5] * sc), pk(a[8 * g + 6] * sc, a[8 * g + 7] * sc)};
            __builtin_amdgcn_raw_buffer_store_b128(dv, rk, vo, (j * 16 + nw * 8 + t * 4 + g * 2) * SLOT, SC1);
          }
      }
      acc[0] = f16v{}; acc[1] = f16v{};
      __builtin_amdgcn_sched_barrier(0);
    }
  });
  correct(false, true);
  o.acc[0] = acc[0]; o.acc[1] = acc[1];
}

DEV float gelu(float v) {
  constexpr float c0 = -2.f * 0.7978845608028654f * 1.4426950408889634f, c1 = 0.044715f * c0;
  return v * __builtin_amdgcn_rcpf(1.f + __builtin_amdgcn_exp2f(v * (c0 + c1 * v * v)));
}
DEV void gelu_store(const P1Out& o, char* ws, int w, u32 lane, int rb, int j) {
  const rsrc_t rh = mk(ws + WS_H, SZ_H);
  const int mw = w & 3, nw = w >> 2;
  const u32 vo = j * 2 + nw < 27 ? (lane >> 5) * SLOT + (rb * 128 + 32 * mw + perm32(lane & 31)) * 16 : OOB;
#pragma unroll
  for (int t = 0; t < 2; t++)
#pragma unroll
    for (int g = 0; g < 2; g++) {
      u32x4 dv;
#pragma unroll
      for (int i = 0; i < 4; i++) dv[i] = pk(gelu(o.acc[t][8 * g + 2 * i] * o.a2), gelu(o.acc[t][8 * g + 2 * i + 1] * o.a2));
      __builtin_amdgcn_raw_buffer_store_b128(dv, rh, vo, (j * 16 + nw * 8 + t * 4 + g * 2) * SLOT, SC1);
    }
}

DEV void transpose_w(const void* w2, const void* wo, char* ws, u64 tag, int w, u32 lane, int rb) {
  const rsrc_t r2 = mk(w2, 1728 * 1280), ro = mk(wo, 640 * 1280), rw = mk(ws + WS_W, SZ_W);
  const u32 r4 = lane >> 4, h = lane >> 5, q = (lane & 15) >> 2;
  const u32 vs = r4 * 1280 + (((lane & 15) ^ (r4 << 2)) << 4);
  const u32 ar = w * 20480 + (8 * h + q) * 256 + (q << 6) + ((lane >> 4) & 1) * 32 + (lane & 3) * 8;
  const u32 vd = (h * 640 + (lane & 31)) * 16;
  auto issue = [&](int i) {
    const int u = rb * 8 + w + 128 * i, kb = u / 5, cb = u - kb * 5;
    const u32 v = u < 740 ? vs : OOB;
#pragma unroll
    for (int k = 0; k < 4; k++) {
      if (kb < 108) dma(r2, v, kb * 20480 + k * 5120 + cb * 256, w * 20480 + (i % 5) * 4096 + k * 1024);
      else dma(ro, v, (kb - 108) * 20480 + k * 5120 + cb * 256, w * 20480 + (i % 5) * 4096 + k * 1024);
    }
  };
#pragma unroll
  for (int i = 0; i < 5; i++) issue(i);
#pragma unroll
  for (int i = 0; i < 6; i++) {
    if (i == 0 || i == 5) wait_vm<16>(); else wait_vm<20>();
    const int u = rb * 8 + w + 128 * i, kb = u / 5, cb = u - kb * 5;
    const u32 v = u < 740 ? vd : OOB;
#pragma unroll
    for (int t = 0; t < 4; t++)
      __builtin_amdgcn_raw_buffer_store_b128(trfrag((ar ^ (t << 6)) + (i % 5) * 4096), rw, v + t * 512, kb * 20480 + cb * 2048, SC1);
    if (i == 0) issue(5);
  }
  wait_bar<0>();
  if (threadIdx.x == 0) stflag(ws + F_QKV + (15 * 16 + rb) * 8, tag);
}

DEV float xhalf_max(float v) {
  u32 b = __builtin_bit_cast(u32, v);
  auto r = __builtin_amdgcn_permlane32_swap(b, b, false, false);
  return fmaxf(__builtin_bit_cast(float, r[0]), __builtin_bit_cast(float, r[1]));
}
DEV f32v score_tile(u32x4 kf, u32x2 kr, u32x4 q0, u32x4 q1, u32x2 qr) {
  f32v s = __builtin_amdgcn_mfma_f32_32x32x4bf16_1k(__builtin_bit_cast(s16x4, kr), __builtin_bit_cast(s16x4, qr), f32v{}, 1, 0, 0);
  f16v a = __builtin_shufflevector(s, s, 0, 1, 2, 3, 4, 5, 6, 7, 8, 9, 10, 11, 12, 13, 14, 15);
  f16v b = __builtin_shufflevector(s, s, 16, 17, 18, 19, 20, 21, 22, 23, 24, 25, 26, 27, 28, 29, 30, 31);
  a = mfma32(kf, q0, a); b = mfma32(kf, q1, b);
  return __builtin_shufflevector(a, b, 0, 1, 2, 3, 4, 5, 6, 7, 8, 9, 10, 11, 12, 13, 14, 15, 16, 17, 18, 19, 20, 21, 22, 23, 24, 25, 26, 27, 28, 29, 30, 31);
}

struct AttIn { u32x4 q0, q1, r0, r1; u32x2 qr; };

DEV void attention(char* ws, u64 tag, int w, u32 lane, int head, int qb, int sa, u32 remoff, const AttIn& in) {
  const rsrc_t rk = mk(ws + WS_QK, SZ_QK);
  const u32 h = lane >> 5, l31 = lane & 31, odd = head & 1, pi = l31 >> 2;
  const u32 dch = pi < 4 ? swap2(pi) + odd : (odd ? 0 : 4);
  const u32 av0 = L_V + (pi < 5 ? (h * 20 + dch * 4 + (l31 & 3)) * 16 : 1280);
  const u32 av1 = av0 + (pi < 5 ? 640 : 0);
  f16v o0, o1;
  for (int attempt = 0;; attempt++) {
    float m0 = -__builtin_inff(), m1 = m0;
    f32v s;
    for (int t = attempt ? 63 : 0; t >= 0; t--) {
      u32x2 kr = {in.r0[0], in.r0[1]};
      if (t) kr = __builtin_amdgcn_raw_buffer_load_b64(rk, l31 * 16, 80 * SLOT + remoff + t * 512, SC1);
      s = score_tile(lds128(L_K + t * 1024 + lane * 16), kr, in.q0, in.q1, in.qr);
#pragma unroll
      for (int i = 0; i < 16; i++) { m0 = fmaxf(m0, s[i]); m1 = fmaxf(m1, s[16 + i]); }
    }
    m0 = xhalf_max(m0); m1 = xhalf_max(m1);
    f32v neg;
#pragma unroll
    for (int i = 0; i < 16; i++) { neg[i] = -m0; neg[16 + i] = -m1; }
    s += neg;
    u32x4 r0 = in.r0, r1 = in.r1;
    u32 ak = L_K + lane * 16, a0 = av0, a1 = av1, ar = L_R + lane * 16, cnt;
    asm volatile(
    "ds_read_b128 v[208:211], v225\n"
    "ds_read_b128 v[204:207], v224 offset:1024\n"
    "v_mov_b32 v160, 0\n"
    "v_mov_b32 v161, 0\n"
    "v_mov_b32 v162, 0\n"
    "v_mov_b32 v163, 0\n"
    "v_mov_b32 v164, 0\n"
    "v_mov_b32 v165, 0\n"
    "v_mov_b32 v166, 0\n"
    "v_mov_b32 v167, 0\n"
    "v_mov_b32 v168, 0\n"
    "v_mov_b32 v169, 0\n"
    "v_mov_b32 v170, 0\n"
    "v_mov_b32 v171, 0\n"
    "v_mov_b32 v172, 0\n"
    "v_mov_b32 v173, 0\n"
    "v_mov_b32 v174, 0\n"
    "v_mov_b32 v175, 0\n"
    "v_mov_b32 v176, 0\n"
    "v_mov_b32 v177, 0\n"
    "v_mov_b32 v178, 0\n"
    "v_mov_b32 v179, 0\n"
    "v_mov_b32 v180, 0\n"
    "v_mov_b32 v181, 0\n"
    "v_mov_b32 v182, 0\n"
    "v_mov_b32 v183, 0\n"
    "v_mov_b32 v184, 0\n"
    "v_mov_b32 v185, 0\n"
    "v_mov_b32 v186, 0\n"
    "v_mov_b32 v187, 0\n"
    "v_mov_b32 v188, 0\n"
    "v_mov_b32 v189, 0\n"
    "v_mov_b32 v190, 0\n"
    "v_mov_b32 v191, 0\n"
    "v_mov_b32 v120, 0\n"
    "v_mov_b32 v121, 0\n"
    "v_mov_b32 v122, 0\n"
    "v_mov_b32 v123, 0\n"
    "v_mov_b32 v124, 0\n"
    "v_mov_b32 v125, 0\n"
    "v_mov_b32 v126, 0\n"
    "v_mov_b32 v127, 0\n"
    "v_mov_b32 v212, 0\n"
    "v_mov_b32 v213, 0\n"
    "v_mov_b32 v214, 0\n"
    "v_mov_b32 v215, 0\n"
    "v_mov_b32 v228, 0xdddddddd\n"
    "s_mov_b32 %[cnt], 8\n"
    "v_exp_f32 v64, v64\n"
    "v_exp_f32 v65, v65\n"
    "1:\n"
    "v_smfmac_f32_32x32x32_bf16 v[176:191], v[212:215], v[120:127], v228\n"
    "s_waitcnt lgkmcnt(1)\n"
    "ds_read_b128 v[212:215], v226 offset:0\n"
    "v_exp_f32 v66, v66\n"
    "v_exp_f32 v67, v67\n"
    "v_exp_f32 v68, v68\n"
    "v_exp_f32 v69, v69\n"
    "v_mfma_f32_32x32x4_2b_bf16 v[96:127], v[216:217], v[200:201], v[128:159] cbsz:1 abid:1\n"
    "v_exp_f32 v70, v70\n"
    "v_exp_f32 v71, v71\n"
    "v_exp_f32 v72, v72\n"
    "v_exp_f32 v73, v73\n"
    "v_exp_f32 v74, v74\n"
    "v_exp_f32 v75, v75\n"
    "v_exp_f32 v76, v76\n"
    "v_exp_f32 v77, v77\n"
    "v_smfmac_f32_32x32x32_bf16 v[160:175], v[208:211], v[64:71], v228\n"
    "s_waitcnt lgkmcnt(0)\n"
    "v_exp_f32 v78, v78\n"
    "v_exp_f32 v79, v79\n"
    "v_exp_f32 v80, v80\n"
    "v_exp_f32 v81, v81\n"
    "v_smfmac_f32_32x32x32_bf16 v[160:175], v[212:215], v[72:79], v228\n"
    "v_exp_f32 v82, v82\n"
    "v_exp_f32 v83, v83\n"
    "v_exp_f32 v84, v84\n"
    "v_exp_f32 v85, v85\n"
    "v_mfma_f32_32x32x16_bf16 v[96:111], v[204:207], v[192:195], v[96:111]\n"
    "v_exp_f32 v86, v86\n"
    "v_exp_f32 v87, v87\n"
    "v_exp_f32 v88, v88\n"
    "v_exp_f32 v89, v89\n"
    "v_smfmac_f32_32x32x32_bf16 v[176:191], v[208:211], v[80:87], v228\n"
    "ds_read_b128 v[208:211], v225 offset:1296\n"
    "v_exp_f32 v90, v90\n"
    "v_exp_f32 v91, v91\n"
    "v_exp_f32 v92, v92\n"
    "v_exp_f32 v93, v93\n"
    "v_mfma_f32_32x32x16_bf16 v[112:127], v[204:207], v[196:199], v[112:127]\n"
    "ds_read_b128 v[204:207], v224 offset:2048\n"
    "v_exp_f32 v94, v94\n"
    "v_exp_f32 v95, v95\n"
    "v_exp_f32 v96, v96\n"
    "v_exp_f32 v97, v97\n"
    "v_smfmac_f32_32x32x32_bf16 v[176:191], v[212:215], v[88:95], v228\n"
    "s_waitcnt lgkmcnt(1)\n"
    "ds_read_b128 v[212:215], v226 offset:1296\n"
    "v_exp_f32 v98, v98\n"
    "v_exp_f32 v99, v99\n"
    "v_exp_f32 v100, v100\n"
    "v_exp_f32 v101, v101\n"
    "v_mfma_f32_32x32x4_2b_bf16 v[64:95], v[218:219], v[200:201], v[128:159] cbsz:1 abid:0\n"
    "v_exp_f32 v102, v102\n"
    "v_exp_f32 v103, v103\n"
    "v_exp_f32 v104, v104\n"
    "v_exp_f32 v105, v105\n"
    "v_exp_f32 v106, v106\n"
    "v_exp_f32 v107, v107\n"
    "v_exp_f32 v108, v108\n"
    "v_exp_f32 v109, v109\n"
    "v_smfmac_f32_32x32x32_bf16 v[160:175], v[208:211], v[96:103], v228\n"
    "s_waitcnt lgkmcnt(0)\n"
    "v_exp_f32 v110, v110\n"
    "v_exp_f32 v111, v111\n"
    "v_exp_f32 v112, v112\n"
    "v_exp_f32 v113, v113\n"
    "v_smfmac_f32_32x32x32_bf16 v[160:175], v[212:215], v[104:111], v228\n"
    "v_exp_f32 v114, v114\n"
    "v_exp_f32 v115, v115\n"
    "v_exp_f32 v116, v116\n"
    "v_exp_f32 v117, v117\n"
    "v_mfma_f32_32x32x16_bf16 v[64:79], v[204:207], v[192:195], v[64:79]\n"
    "v_exp_f32 v118, v118\n"
    "v_exp_f32 v119, v119\n"
    "v_exp_f32 v120, v120\n"
    "v_exp_f32 v121, v121\n"
    "v_smfmac_f32_32x32x32_bf16 v[176:191], v[208:211], v[112:119], v228\n"
    "ds_read_b128 v[208:211], v225 offset:2592\n"
    "v_exp_f32 v122, v122\n"
    "v_exp_f32 v123, v123\n"
    "v_exp_f32 v124, v124\n"
    "v_exp_f32 v125, v125\n"
    "v_mfma_f32_32x32x16_bf16 v[80:95], v[204:207], v[196:199], v[80:95]\n"
    "ds_read_b128 v[204:207], v224 offset:3072\n"
    "v_exp_f32 v126, v126\n"
    "v_exp_f32 v127, v127\n"
    "v_exp_f32 v64, v64\n"
    "v_exp_f32 v65, v65\n"
    "v_smfmac_f32_32x32x32_bf16 v[176:191], v[212:215], v[120:127], v228\n"
    "s_waitcnt lgkmcnt(1)\n"
    "ds_read_b128 v[212:215], v226 offset:2592\n"
    "v_exp_f32 v66, v66\n"
    "v_exp_f32 v67, v67\n"
    "v_exp_f32 v68, v68\n"
    "v_exp_f32 v69, v69\n"
    "v_mfma_f32_32x32x4_2b_bf16 v[96:127], v[218:219], v[200:201], v[128:159] cbsz:1 abid:1\n"
    "v_exp_f32 v70, v70\n"
    "v_exp_f32 v71, v71\n"
    "v_exp_f32 v72, v72\n"
    "v_exp_f32 v73, v73\n"
    "v_exp_f32 v74, v74\n"
    "v_exp_f32 v75, v75\n"
    "v_exp_f32 v76, v76\n"
    "v_exp_f32 v77, v77\n"
    "v_smfmac_f32_32x32x32_bf16 v[160:175], v[208:211], v[64:71], v228\n"
    "s_waitcnt lgkmcnt(0)\n"
    "v_exp_f32 v78, v78\n"
    "v_exp_f32 v79, v79\n"
    "v_exp_f32 v80, v80\n"
    "v_exp_f32 v81, v81\n"
    "v_smfmac_f32_32x32x32_bf16 v[160:175], v[212:215], v[72:79], v228\n"
    "ds_read_b128 v[216:219], v227 offset:0\n"
    "v_exp_f32 v82, v82\n"
    "v_exp_f32 v83, v83\n"
    "v_exp_f32 v84, v84\n"
    "v_exp_f32 v85, v85\n"
    "v_mfma_f32_32x32x16_bf16 v[96:111], v[204:207], v[192:195], v[96:111]\n"
    "v_exp_f32 v86, v86\n"
    "v_exp_f32 v87, v87\n"
    "v_exp_f32 v88, v88\n"
    "v_exp_f32 v89, v89\n"
    "v_smfmac_f32_32x32x32_bf16 v[176:191], v[208:211], v[80:87], v228\n"
    "ds_read_b128 v[208:211], v225 offset:3888\n"
    "v_exp_f32 v90, v90\n"
    "v_exp_f32 v91, v91\n"
    "v_exp_f32 v92, v92\n"
    "v_exp_f32 v93, v93\n"
    "v_mfma_f32_32x32x16_bf16 v[112:127], v[204:207], v[196:199], v[112:127]\n"
    "ds_read_b128 v[204:207], v224 offset:4096\n"
    "v_exp_f32 v94, v94\n"
    "v_exp_f32 v95, v95\n"
    "v_exp_f32 v96, v96\n"
    "v_exp_f32 v97, v97\n"
    "v_smfmac_f32_32x32x32_bf16 v[176:191], v[212:215], v[88:95], v228\n"
    "s_waitcnt lgkmcnt(1)\n"
    "ds_read_b128 v[212:215], v226 offset:3888\n"
    "v_exp_f32 v98, v98\n"
    "v_exp_f32 v99, v99\n"
    "v_exp_f32 v100, v100\n"
    "v_exp_f32 v101, v101\n"
    "v_mfma_f32_32x32x4_2b_bf16 v[64:95], v[220:221], v[200:201], v[128:159] cbsz:1 abid:0\n"
    "v_exp_f32 v102, v102\n"
    "v_exp_f32 v103, v103\n"
    "v_exp_f32 v104, v104\n"
    "v_exp_f32 v105, v105\n"
    "v_exp_f32 v106, v106\n"
    "v_exp_f32 v107, v107\n"
    "v_exp_f32 v108, v108\n"
    "v_exp_f32 v109, v109\n"
    "v_smfmac_f32_32x32x32_bf16 v[160:175], v[208:211], v[96:103], v228\n"
    "s_waitcnt lgkmcnt(0)\n"
    "v_exp_f32 v110, v110\n"
    "v_exp_f32 v111, v111\n"
    "v_exp_f32 v112, v112\n"
    "v_exp_f32 v113, v113\n"
    "v_smfmac_f32_32x32x32_bf16 v[160:175], v[212:215], v[104:111], v228\n"
    "v_exp_f32 v114, v114\n"
    "v_exp_f32 v115, v115\n"
    "v_exp_f32 v116, v116\n"
    "v_exp_f32 v117, v117\n"
    "v_mfma_f32_32x32x16_bf16 v[64:79], v[204:207], v[192:195], v[64:79]\n"
    "v_exp_f32 v118, v118\n"
    "v_exp_f32 v119, v119\n"
    "v_exp_f32 v120, v120\n"
    "v_exp_f32 v121, v121\n"
    "v_smfmac_f32_32x32x32_bf16 v[176:191], v[208:211], v[112:119], v228\n"
    "ds_read_b128 v[208:211], v225 offset:5184\n"
    "v_exp_f32 v122, v122\n"
    "v_exp_f32 v123, v123\n"
    "v_exp_f32 v124, v124\n"
    "v_exp_f32 v125, v125\n"
    "v_mfma_f32_32x32x16_bf16 v[80:95], v[204:207], v[196:199], v[80:95]\n"
    "ds_read_b128 v[204:207], v224 offset:5120\n"
    "v_exp_f32 v126, v126\n"
    "v_exp_f32 v127, v127\n"
    "v_exp_f32 v64, v64\n"
    "v_exp_f32 v65, v65\n"
    "v_smfmac_f32_32x32x32_bf16 v[176:191], v[212:215], v[120:127], v228\n"
    "s_waitcnt lgkmcnt(1)\n"
    "ds_read_b128 v[212:215], v226 offset:5184\n"
    "v_exp_f32 v66, v66\n"
    "v_exp_f32 v67, v67\n"
    "v_exp_f32 v68, v68\n"
    "v_exp_f32 v69, v69\n"
    "v_mfma_f32_32x32x4_2b_bf16 v[96:127], v[220:221], v[200:201], v[128:159] cbsz:1 abid:1\n"
    "v_exp_f32 v70, v70\n"
    "v_exp_f32 v71, v71\n"
    "v_exp_f32 v72, v72\n"
    "v_exp_f32 v73, v73\n"
    "v_exp_f32 v74, v74\n"
    "v_exp_f32 v75, v75\n"
    "v_exp_f32 v76, v76\n"
    "v_exp_f32 v77, v77\n"
    "v_smfmac_f32_32x32x32_bf16 v[160:175], v[208:211], v[64:71], v228\n"
    "s_waitcnt lgkmcnt(0)\n"
    "v_exp_f32 v78, v78\n"
    "v_exp_f32 v79, v79\n"
    "v_exp_f32 v80, v80\n"
    "v_exp_f32 v81, v81\n"
    "v_smfmac_f32_32x32x32_bf16 v[160:175], v[212:215], v[72:79], v228\n"
    "v_exp_f32 v82, v82\n"
    "v_exp_f32 v83, v83\n"
    "v_exp_f32 v84, v84\n"
    "v_exp_f32 v85, v85\n"
    "v_mfma_f32_32x32x16_bf16 v[96:111], v[204:207], v[192:195], v[96:111]\n"
    "v_exp_f32 v86, v86\n"
    "v_exp_f32 v87, v87\n"
    "v_exp_f32 v88, v88\n"
    "v_exp_f32 v89, v89\n"
    "v_smfmac_f32_32x32x32_bf16 v[176:191], v[208:211], v[80:87], v228\n"
    "ds_read_b128 v[208:211], v225 offset:6480\n"
    "v_exp_f32 v90, v90\n"
    "v_exp_f32 v91, v91\n"
    "v_exp_f32 v92, v92\n"
    "v_exp_f32 v93, v93\n"
    "v_mfma_f32_32x32x16_bf16 v[112:127], v[204:207], v[196:199], v[112:127]\n"
    "ds_read_b128 v[204:207], v224 offset:6144\n"
    "v_exp_f32 v94, v94\n"
    "v_exp_f32 v95, v95\n"
    "v_exp_f32 v96, v96\n"
    "v_exp_f32 v97, v97\n"
    "v_smfmac_f32_32x32x32_bf16 v[176:191], v[212:215], v[88:95], v228\n"
    "s_waitcnt lgkmcnt(1)\n"
    "ds_read_b128 v[212:215], v226 offset:6480\n"
    "v_exp_f32 v98, v98\n"
    "v_exp_f32 v99, v99\n"
    "v_exp_f32 v100, v100\n"
    "v_exp_f32 v101, v101\n"
    "v_mfma_f32_32x32x4_2b_bf16 v[64:95], v[222:223], v[200:201], v[128:159] cbsz:1 abid:0\n"
    "v_exp_f32 v102, v102\n"
    "v_exp_f32 v103, v103\n"
    "v_exp_f32 v104, v104\n"
    "v_exp_f32 v105, v105\n"
    "v_exp_f32 v106, v106\n"
    "v_exp_f32 v107, v107\n"
    "v_exp_f32 v108, v108\n"
    "v_exp_f32 v109, v109\n"
    "v_smfmac_f32_32x32x32_bf16 v[160:175], v[208:211], v[96:103], v228\n"
    "s_waitcnt lgkmcnt(0)\n"
    "v_exp_f32 v110, v110\n"
    "v_exp_f32 v111, v111\n"
    "v_exp_f32 v112, v112\n"
    "v_exp_f32 v113, v113\n"
    "v_smfmac_f32_32x32x32_bf16 v[160:175], v[212:215], v[104:111], v228\n"
    "v_exp_f32 v114, v114\n"
    "v_exp_f32 v115, v115\n"
    "v_exp_f32 v116, v116\n"
    "v_exp_f32 v117, v117\n"
    "v_mfma_f32_32x32x16_bf16 v[64:79], v[204:207], v[192:195], v[64:79]\n"
    "v_exp_f32 v118, v118\n"
    "v_exp_f32 v119, v119\n"
    "v_exp_f32 v120, v120\n"
    "v_exp_f32 v121, v121\n"
    "v_smfmac_f32_32x32x32_bf16 v[176:191], v[208:211], v[112:119], v228\n"
    "ds_read_b128 v[208:211], v225 offset:7776\n"
    "v_exp_f32 v122, v122\n"
    "v_exp_f32 v123, v123\n"
    "v_exp_f32 v124, v124\n"
    "v_exp_f32 v125, v125\n"
    "v_mfma_f32_32x32x16_bf16 v[80:95], v[204:207], v[196:199], v[80:95]\n"
    "ds_read_b128 v[204:207], v224 offset:7168\n"
    "v_exp_f32 v126, v126\n"
    "v_exp_f32 v127, v127\n"
    "v_exp_f32 v64, v64\n"
    "v_exp_f32 v65, v65\n"
    "v_smfmac_f32_32x32x32_bf16 v[176:191], v[212:215], v[120:127], v228\n"
    "s_waitcnt lgkmcnt(1)\n"
    "ds_read_b128 v[212:215], v226 offset:7776\n"
    "v_exp_f32 v66, v66\n"
    "v_exp_f32 v67, v67\n"
    "v_exp_f32 v68, v68\n"
    "v_exp_f32 v69, v69\n"
    "v_mfma_f32_32x32x4_2b_bf16 v[96:127], v[222:223], v[200:201], v[128:159] cbsz:1 abid:1\n"
    "v_exp_f32 v70, v70\n"
    "v_exp_f32 v71, v71\n"
    "v_exp_f32 v72, v72\n"
    "v_exp_f32 v73, v73\n"
    "v_exp_f32 v74, v74\n"
    "v_exp_f32 v75, v75\n"
    "v_exp_f32 v76, v76\n"
    "v_exp_f32 v77, v77\n"
    "v_smfmac_f32_32x32x32_bf16 v[160:175], v[208:211], v[64:71], v228\n"
    "s_waitcnt lgkmcnt(0)\n"
    "v_exp_f32 v78, v78\n"
    "v_exp_f32 v79, v79\n"
    "v_exp_f32 v80, v80\n"
    "v_exp_f32 v81, v81\n"
    "v_smfmac_f32_32x32x32_bf16 v[160:175], v[212:215], v[72:79], v228\n"
    "ds_read_b128 v[220:223], v227 offset:1024\n"
    "v_exp_f32 v82, v82\n"
    "v_exp_f32 v83, v83\n"
    "v_exp_f32 v84, v84\n"
    "v_exp_f32 v85, v85\n"
    "v_mfma_f32_32x32x16_bf16 v[96:111], v[204:207], v[192:195], v[96:111]\n"
    "v_exp_f32 v86, v86\n"
    "v_exp_f32 v87, v87\n"
    "v_exp_f32 v88, v88\n"
    "v_exp_f32 v89, v89\n"
    "v_smfmac_f32_32x32x32_bf16 v[176:191], v[208:211], v[80:87], v228\n"
    "ds_read_b128 v[208:211], v225 offset:9072\n"
    "v_exp_f32 v90, v90\n"
    "v_exp_f32 v91, v91\n"
    "v_exp_f32 v92, v92\n"
    "v_exp_f32 v93, v93\n"
    "v_mfma_f32_32x32x16_bf16 v[112:127], v[204:207], v[196:199], v[112:127]\n"
    "ds_read_b128 v[204:207], v224 offset:8192\n"
    "v_exp_f32 v94, v94\n"
    "v_exp_f32 v95, v95\n"
    "v_exp_f32 v96, v96\n"
    "v_exp_f32 v97, v97\n"
    "v_smfmac_f32_32x32x32_bf16 v[176:191], v[212:215], v[88:95], v228\n"
    "s_waitcnt lgkmcnt(1)\n"
    "ds_read_b128 v[212:215], v226 offset:9072\n"
    "v_exp_f32 v98, v98\n"
    "v_exp_f32 v99, v99\n"
    "v_exp_f32 v100, v100\n"
    "v_exp_f32 v101, v101\n"
    "v_mfma_f32_32x32x4_2b_bf16 v[64:95], v[216:217], v[200:201], v[128:159] cbsz:1 abid:0\n"
    "v_exp_f32 v102, v102\n"
    "v_exp_f32 v103, v103\n"
    "v_exp_f32 v104, v104\n"
    "v_exp_f32 v105, v105\n"
    "v_exp_f32 v106, v106\n"
    "v_exp_f32 v107, v107\n"
    "v_exp_f32 v108, v108\n"
    "v_exp_f32 v109, v109\n"
    "v_smfmac_f32_32x32x32_bf16 v[160:175], v[208:211], v[96:103], v228\n"
    "s_waitcnt lgkmcnt(0)\n"
    "v_exp_f32 v110, v110\n"
    "v_exp_f32 v111, v111\n"
    "v_exp_f32 v112, v112\n"
    "v_exp_f32 v113, v113\n"
    "v_smfmac_f32_32x32x32_bf16 v[160:175], v[212:215], v[104:111], v228\n"
    "v_exp_f32 v114, v114\n"
    "v_exp_f32 v115, v115\n"
    "v_exp_f32 v116, v116\n"
    "v_exp_f32 v117, v117\n"
    "v_mfma_f32_32x32x16_bf16 v[64:79], v[204:207], v[192:195], v[64:79]\n"
    "s_sub_u32 %[cnt], %[cnt], 1\n"
    "v_exp_f32 v118, v118\n"
    "v_exp_f32 v119, v119\n"
    "v_exp_f32 v120, v120\n"
    "v_exp_f32 v121, v121\n"
    "v_smfmac_f32_32x32x32_bf16 v[176:191], v[208:211], v[112:119], v228\n"
    "ds_read_b128 v[208:211], v225 offset:10368\n"
    "v_exp_f32 v122, v122\n"
    "v_exp_f32 v123, v123\n"
    "v_exp_f32 v124, v124\n"
    "v_exp_f32 v125, v125\n"
    "v_mfma_f32_32x32x16_bf16 v[80:95], v[204:207], v[196:199], v[80:95]\n"
    "ds_read_b128 v[204:207], v224 offset:9216\n"
    "v_exp_f32 v126, v126\n"
    "v_exp_f32 v127, v127\n"
    "v_add_u32 v224, 8192, v224\n"
    "v_add_u32 v225, 10368, v225\n"
    "v_add_u32 v226, 10368, v226\n"
    "v_add_u32 v227, 2048, v227\n"
    "s_cmp_lg_u32 %[cnt], 0\n"
    "v_exp_f32 v64, v64\n"
    "v_exp_f32 v65, v65\n"
    "s_cbranch_scc1 1b\n"
    "v_smfmac_f32_32x32x32_bf16 v[176:191], v[212:215], v[120:127], v228\n"
    "s_waitcnt lgkmcnt(0)\n"
    "s_nop 11\n"
        : "+{v[64:95]}"(s), "=&{v[160:175]}"(o0), "=&{v[176:191]}"(o1), "+{v[216:219]}"(r0), "+{v[220:223]}"(r1),
          "+{v224}"(ak), "+{v225}"(a0), "+{v226}"(a1), "+{v227}"(ar), [cnt] "=&s"(cnt)
        : "{v[128:159]}"(neg), "{v[192:195]}"(in.q0), "{v[196:199]}"(in.q1), "{v[200:201]}"(in.qr)
        : "v96", "v97", "v98", "v99", "v100", "v101", "v102", "v103", "v104", "v105", "v106", "v107", "v108", "v109", "v110", "v111",
          "v112", "v113", "v114", "v115", "v116", "v117", "v118", "v119", "v120", "v121", "v122", "v123", "v124", "v125", "v126", "v127",
          "v204", "v205", "v206", "v207", "v208", "v209", "v210", "v211", "v212", "v213", "v214", "v215", "v228", "scc", "memory");
    if (attempt || __all(o0[12] < 0x1p64f && o1[12] < 0x1p64f)) break;
  }
  const rsrc_t ro = mk(ws + WS_O, SZ_O);
  const u32 tok = (qb * 256 + w * 64 + l31) * 16;
#pragma unroll
  for (int b = 0; b < 2; b++) {
    const f16v& o = b ? o1 : o0;
    const float r = __builtin_amdgcn_rcpf(o[12]);
    u32x4 dv = {pk(o[0] * r, o[1] * r), pk(o[2] * r, o[3] * r), pk(o[4] * r, o[5] * r), pk(o[6] * r, o[7] * r)};
    u32x2 dr = {pk(o[8] * r, o[9] * r), pk(o[10] * r, o[11] * r)};
    __builtin_amdgcn_raw_buffer_store_b128(dv, ro, h * SLOT + tok + b * 512, sa * SLOT, SC1);
    __builtin_amdgcn_raw_buffer_store_b64(dr, ro, h ? OOB : tok + b * 512, remoff, SC1);
  }
  wait_vm<0>();
  if (lane == 0) stflag(ws + F_O + ((qb * 4 + w) * 32 + head) * 8, tag);
}

DEV void mlp2(char* ws, u64 tag, int kq, u32 lane, int mb, int nb, f4 (&acc)[4][5]) {
  const rsrc_t rw = mk(ws + WS_W, SZ_W), rh = mk(ws + WS_H, SZ_H);
  poll(ws + (lane < 16 ? F_QKV + (15 * 16 + lane) * 8 : F_H + ((mb >> 1) * 16 + (lane < 30 ? lane - 16 : 0)) * 8), tag);
  const u32 g = lane >> 4, l15 = lane & 15;
  const u32 va = (g * 640 + nb * 80 + l15) * 16, vb = g * SLOT + (mb * 64 + l15) * 16;
  const u32 vat = kq < 3 ? va : OOB, vbt = kq < 3 ? vb : OOB;
  constexpr int D = 3;
  u32x4 A[D][5], B[D][4];
  auto ld = [&](auto I) {
    constexpr int i = I;
#pragma unroll
    for (int n = 0; n < 5; n++) A[i % D][n] = __builtin_amdgcn_raw_buffer_load_b128(rw, (i < 12 ? va : vat) + n * 256, (kq * 14 + i) * 40960, SC1);
#pragma unroll
    for (int m = 0; m < 4; m++) B[i % D][m] = __builtin_amdgcn_raw_buffer_load_b128(rh, (i < 12 ? vb : vbt) + m * 256, (kq * 14 + i) * 4 * SLOT, SC1);
  };
  sfor<0, D>(ld);
  sfor<0, 14>([&](auto I) {
    constexpr int i = I;
#pragma unroll
    for (int m = 0; m < 4; m++)
#pragma unroll
      for (int n = 0; n < 5; n++) acc[m][n] = mfma16(A[i % D][n], B[i % D][m], i ? acc[m][n] : f4{});
    __builtin_amdgcn_sched_barrier(0);
    if constexpr (i + D < 14) ld(std::integral_constant<int, i + D>{});
    __builtin_amdgcn_sched_barrier(0);
  });
}

__global__ void __launch_bounds__(512, 2)
fused_block(const void* x, const void* wqkv, const void* wo, const void* w1, const void* b1, const void* w2, const void* b2, void* out, char* ws) {
  __shared__ __attribute__((aligned(16))) char lds[163840];
  asm volatile("" ::"v"((u32)(size_t)(LDSP(char))lds) : "memory");
  const u32 lane = threadIdx.x & 63;
  const int w = __builtin_amdgcn_readfirstlane(threadIdx.x >> 6);
  const int xcd = blockIdx.x & 7, r = blockIdx.x >> 3;
  const u64 tag = ldflag(ws + F_NONCE) * 6364136223846793005ull + 1442695040888963407ull;

  const int rb = (xcd & 1) * 8 + (r & 7), j = (xcd >> 1) * 4 + (r >> 3);
  P1Out p1;
  if (j == 15) { transpose_w(w2, wo, ws, tag, w, lane, rb); p1 = P1Out{}; }
  else if (j >= 10) phase1<true>(x, wqkv, w1, b1, ws, tag, w, lane, rb, j, r % 10, p1);
  else phase1<false>(x, wqkv, w1, b1, ws, tag, w, lane, rb, j, r % 10, p1);

  const int head = xcd * 4 + (r & 3), qb = r >> 2;
  const int sa = (5 * head + 1) >> 1;
  const u32 remoff = head & 1 ? (sa - 1) * SLOT + 8 : (sa + 2) * SLOT;
  if (w == 0) {
    const u32 jj = ((lane >> 4) & 1 ? head * 20 + 19 : head * 20) >> 7;
    poll(ws + F_QKV + ((5 + 5 * (lane >> 5) + jj) * 16 + (lane & 15)) * 8, tag);
    poll(ws + F_QKV + (jj * 16 + qb * 2 + (lane & 1)) * 8, tag);
  }
  lds_bar();
  const rsrc_t rk = mk(ws + WS_QK, SZ_QK);
  {
    const rsrc_t rv = mk(ws + WS_V, SZ_V);
    const u32 vk = (lane >> 5) * SLOT + (lane & 31) * 16;
#pragma unroll
    for (int i = 0; i < 8; i++) {
      dma_sc1(rk, vk, (80 + sa) * SLOT + (i * 8 + w) * 512, L_K + (i * 8 + w) * 1024);
      dma_sc1(rv, lane * 16, head * VHEAD + (i * 8 + w) * 1024, L_V + (i * 8 + w) * 1024);
    }
#pragma unroll
    for (int i = 8; i < 10; i++) dma_sc1(rv, lane * 16, head * VHEAD + (i * 8 + w) * 1024, L_V + (i * 8 + w) * 1024);
    if (w == 0) dma_sc1(rv, lane * 16, head * VHEAD + 80 * 1024, L_V + 80 * 1024);
  }
  AttIn ai;
  u32x4 rq[2];
  {
#pragma unroll
    for (int i = 0; i < 2; i++) {
      u32x2 a = __builtin_amdgcn_raw_buffer_load_b64(rk, lane * 16, 80 * SLOT + remoff + (w * 2 + i) * 2048, SC1);
      u32x2 b = __builtin_amdgcn_raw_buffer_load_b64(rk, lane * 16 + 1024, 80 * SLOT + remoff + (w * 2 + i) * 2048, SC1);
      rq[i] = u32x4{a[0], a[1], b[0], b[1]};
    }
    if (w < 4) {
      const u32 vq = (lane >> 5) * SLOT + (lane & 31) * 16, q0 = (qb * 256 + w * 64) * 16;
      ai.q0 = __builtin_amdgcn_raw_buffer_load_b128(rk, vq, sa * SLOT + q0, SC1);
      ai.q1 = __builtin_amdgcn_raw_buffer_load_b128(rk, vq + 512, sa * SLOT + q0, SC1);
      ai.qr = __builtin_amdgcn_raw_buffer_load_b64(rk, lane * 16, remoff + q0, SC1);
#pragma unroll
      for (int i = 0; i < 2; i++) {
        u32x2 a = __builtin_amdgcn_raw_buffer_load_b64(rk, lane * 16 + i * 2048, 80 * SLOT + remoff, SC1);
        u32x2 b = __builtin_amdgcn_raw_buffer_load_b64(rk, lane * 16 + i * 2048 + 1024, 80 * SLOT + remoff, SC1);
        (i ? ai.r1 : ai.r0) = u32x4{a[0], a[1], b[0], b[1]};
      }
    }
  }
  __builtin_amdgcn_sched_barrier(0);
  if (j < 14) gelu_store(p1, ws, w, lane, rb, j);
  __builtin_amdgcn_sched_barrier(0);
  if (w > 0) {
#pragma unroll
    for (int i = 0; i < 2; i++) *(LDSP(u32x4))(L_R + (w * 2 + i - 2) * 1024 + lane * 16) = rq[i];
  }
  wait_bar<0>();
  if (threadIdx.x == 256 && j < 14) stflag(ws + F_H + (rb * 16 + j) * 8, tag);

  const int mb = head, nb = qb;
  if (w < 4) {
    __builtin_amdgcn_s_setprio(3);
    attention(ws, tag, w, lane, head, qb, sa, remoff, ai);
    __builtin_amdgcn_s_setprio(0);
    lds_bar();
  } else {
    f4 acc[4][5];
    mlp2(ws, tag, w - 4, lane, mb, nb, acc);
    lds_bar();
#pragma unroll
    for (int m = 0; m < 4; m++)
#pragma unroll
      for (int n = 0; n < 5; n++) *(LDSP(f4))(((w - 4) * 20 + m * 5 + n) * 1024 + lane * 16) = acc[m][n];
  }

  const u32 g = lane >> 4, l15 = lane & 15;
  const int mh = w & 1, kq = w >> 1;
  u32x4 A[5][5];
  {
    const rsrc_t rw = mk(ws + WS_W, SZ_W);
    const u32 va = (g * 640 + nb * 80 + l15) * 16;
#pragma unroll
    for (int k = 0; k < 5; k++)
#pragma unroll
      for (int n = 0; n < 5; n++) A[k][n] = __builtin_amdgcn_raw_buffer_load_b128(rw, va + n * 256, (54 + kq * 5 + k) * 40960, SC1);
  }
  u32x2 xr[3], br[3];
  u32 vo[3];
#pragma unroll
  for (int i = 0; i < 3; i++) {
    const int u = w + 8 * i, m = u / 5, n = u - m * 5;
    const u32 col = (nb * 80 + n * 16 + g * 4) * 2;
    vo[i] = u < 20 ? (mb * 64 + m * 16 + l15) * 1280 + col : OOB;
    xr[i] = __builtin_amdgcn_raw_buffer_load_b64(mk(x, 2048 * 1280), vo[i], 0, 0);
    br[i] = __builtin_amdgcn_raw_buffer_load_b64(mk(b2, 1280), col, 0, 0);
  }
  lds_bar();
  f4 gate[3];
#pragma unroll
  for (int i = 0; i < 3; i++) {
    const u32 a = (w + 8 * i) * 1024 + lane * 16;
    f4 m = (ldsf4(a) + ldsf4(a + 20480)) + (ldsf4(a + 40960) + ldsf4(a + 61440));
    m += f4{bflo(br[i][0]), bfhi(br[i][0]), bflo(br[i][1]), bfhi(br[i][1])};
#pragma unroll
    for (int e = 0; e < 4; e++) gate[i][e] = m[e] * __builtin_amdgcn_rcpf(1.f + __builtin_amdgcn_exp2f(m[e] * -1.4426950408889634f));
  }
  __builtin_amdgcn_sched_barrier(0);
  poll(ws + F_O + (mb * 32 + kq * 8 + (lane & 7)) * 8, tag);
  {
    const rsrc_t ro = mk(ws + WS_O, SZ_O);
    const u32 vb = g * SLOT + (mb * 64 + mh * 32 + l15) * 16;
    u32x4 B[5][2];
#pragma unroll
    for (int k = 0; k < 5; k++)
#pragma unroll
      for (int m = 0; m < 2; m++) B[k][m] = __builtin_amdgcn_raw_buffer_load_b128(ro, vb + m * 256, (kq * 5 + k) * 4 * SLOT, SC1);
    f4 acc[2][5];
#pragma unroll
    for (int k = 0; k < 5; k++)
#pragma unroll
      for (int m = 0; m < 2; m++)
#pragma unroll
        for (int n = 0; n < 5; n++) acc[m][n] = mfma16(A[k][n], B[k][m], k ? acc[m][n] : f4{});
#pragma unroll
    for (int m = 0; m < 2; m++)
#pragma unroll
      for (int n = 0; n < 5; n++) *(LDSP(f4))(81920 + (kq * 20 + (mh * 2 + m) * 5 + n) * 1024 + lane * 16) = acc[m][n];
  }
  lds_bar();
#pragma unroll
  for (int i = 0; i < 3; i++) {
    const u32 a = 81920 + (w + 8 * i) * 1024 + lane * 16;
    f4 s = (ldsf4(a) + ldsf4(a + 20480)) + (ldsf4(a + 40960) + ldsf4(a + 61440));
    f4 y = f4{bflo(xr[i][0]), bfhi(xr[i][0]), bflo(xr[i][1]), bfhi(xr[i][1])} + s * gate[i];
    __builtin_amdgcn_raw_buffer_store_b64(u32x2{pk(y[0], y[1]), pk(y[2], y[3])}, mk(out, 2048 * 1280), vo[i], 0, 0);
  }
  if (blockIdx.x == 0 && threadIdx.x == 0) stflag(ws + F_NONCE, tag);
}

extern "C" void kernel_launch(void* const* d_in, const int* in_sizes, int n_in, void* d_out, int out_size, void* d_ws, size_t ws_size,
                              hipStream_t stream) {
  fused_block<<<256, 512, 0, stream>>>(d_in[0], d_in[1], d_in[2], d_in[4], d_in[5], d_in[6], d_in[7], d_out, (char*)d_ws);
}
```

```cpp
#include <hip/hip_runtime.h>
#pragma clang diagnostic ignored "-Wint-to-pointer-cast"
#pragma clang diagnostic ignored "-Winline-asm"

typedef unsigned u32;
typedef unsigned long long u64;
typedef u32 u32x2 __attribute__((ext_vector_type(2)));
typedef u32 u32x4 __attribute__((ext_vector_type(4)));
typedef short s16x4 __attribute__((ext_vector_type(4)));
typedef __bf16 bf2 __attribute__((ext_vector_type(2)));
typedef __bf16 bf8 __attribute__((ext_vector_type(8)));
typedef float f2 __attribute__((ext_vector_type(2)));
typedef float f4 __attribute__((ext_vector_type(4)));
typedef float f16v __attribute__((ext_vector_type(16)));
typedef float f32v __attribute__((ext_vector_type(32)));
typedef __amdgpu_buffer_rsrc_t rsrc_t;
#define LDSP(T) __attribute__((address_space(3))) T*
#define GLP(T) __attribute__((address_space(1))) T*
#define DEV __device__ __forceinline__

constexpr u32 SLOT = 2048 * 16;
constexpr u32 F_NONCE = 0, F_QKV = 1024, F_H = 4096, F_ST = 32768;
constexpr u32 WS_QK = 65536, SZ_QK = 160 * SLOT;
constexpr u32 VHEAD = 64 * 1296;
constexpr u32 WS_V = WS_QK + SZ_QK, SZ_V = 32 * VHEAD;
constexpr u32 WS_H = WS_V + SZ_V, SZ_H = 216 * SLOT;
constexpr u32 WS_W = WS_H + SZ_H, SZ_W = 296 * 640 * 16;
constexpr u32 WS_O = WS_W + SZ_W, SZ_O = 80 * SLOT;
constexpr u32 WS_END = WS_O + SZ_O;
constexpr u32 OOB = 0x80000000u;
constexpr int SC1 = 16;
constexpr u32 SPIN_MAX = 1u << 20;

constexpr u32 L_EX = 162816, L_CNT = L_EX + 512;
constexpr u32 L_PM = 0, L_PS = 61440, L_GATE = 122880, L_X = L_GATE + 20480;
constexpr u32 L_K = 0, L_V = 65536, L_R = L_V + VHEAD;

constexpr u32 K_ROT = 0x85308530;
constexpr int TR_DEPTH = 5;
constexpr int KV_N0 = 3, KV_N1 = 5, KV_N2 = 8;
constexpr int XQ = 6, SP = 2;
constexpr int W2_DEPTH = 4;
constexpr int W2_DEFER = 4;
constexpr int O_DEPTH = 10;
constexpr int G0 = 11;
constexpr int G1 = 15;
constexpr int CQ = 87;

DEV rsrc_t mk(const void* p, u32 bytes) { return __builtin_amdgcn_make_buffer_rsrc((void*)p, 0, bytes, 0x00020000); }
DEV void dma(rsrc_t rs, u32 voff, u32 soff, u32 lds) {
  asm volatile("s_nop 0\n\tbuffer_load_dwordx4 %1, %2, %3 offen lds" ::"{m0}"(lds), "v"(voff), "s"(rs), "s"(soff) : "memory");
}
DEV void dma_sc1(rsrc_t rs, u32 voff, u32 soff, u32 lds) {
  asm volatile("s_nop 0\n\tbuffer_load_dwordx4 %1, %2, %3 offen sc1 lds" ::"{m0}"(lds), "v"(voff), "s"(rs), "s"(soff) : "memory");
}
DEV void st128(u32x4 d, rsrc_t rs, u32 voff, u32 soff) {
  __builtin_amdgcn_raw_buffer_store_b128(d, rs, voff, soff, SC1);
  asm volatile("s_nop 1" ::"v"(d) : "memory");
}
template <int N> DEV void wait_bar() { asm volatile("s_waitcnt vmcnt(%0) lgkmcnt(0)\n\ts_barrier" ::"i"(N) : "memory"); }
DEV void lds_bar() { asm volatile("s_waitcnt lgkmcnt(0)\n\ts_barrier" ::: "memory"); }
template <int N> DEV void wait_vm() { asm volatile("s_waitcnt vmcnt(%0)" ::"i"(N) : "memory"); }
DEV u32 lane_id() { u32 l; asm volatile("v_mbcnt_lo_u32_b32 %0, -1, 0\n\tv_mbcnt_hi_u32_b32 %0, -1, %0" : "=v"(l)); return l; }
DEV void keep(u32x4 v) { asm volatile("" ::"v"(v)); }
DEV u32x4 lds128(u32 a) { return *(LDSP(u32x4))a; }
DEV f4 ldsf4(u32 a) { return *(LDSP(f4))a; }
DEV u32x4 trfrag(u32 a) {
  s16x4 lo = __builtin_amdgcn_ds_read_tr16_b64_v4i16((LDSP(s16x4))a);
  s16x4 hi = __builtin_amdgcn_ds_read_tr16_b64_v4i16((LDSP(s16x4))(a + 1024));
  u32x2 l = __builtin_bit_cast(u32x2, lo), h = __builtin_bit_cast(u32x2, hi);
  return u32x4{l[0], l[1], h[0], h[1]};
}
DEV f16v mfma32(u32x4 a, u32x4 b, f16v c) { return __builtin_amdgcn_mfma_f32_32x32x16_bf16(__builtin_bit_cast(bf8, a), __builtin_bit_cast(bf8, b), c, 0, 0, 0); }
DEV f4 mfma16(u32x4 a, u32x4 b, f4 c) { return __builtin_amdgcn_mfma_f32_16x16x32_bf16(__builtin_bit_cast(bf8, a), __builtin_bit_cast(bf8, b), c, 0, 0, 0); }
DEV float dot2(u32 a, u32 b, float c) { return __builtin_amdgcn_fdot2_f32_bf16(__builtin_bit_cast(bf2, a), __builtin_bit_cast(bf2, b), c, false); }
DEV u32 pk(float a, float b) { return __builtin_bit_cast(u32, __builtin_convertvector(f2{a, b}, bf2)); }
DEV float bflo(u32 v) { return __builtin_bit_cast(float, v << 16); }
DEV float bfhi(u32 v) { return __builtin_bit_cast(float, v & 0xffff0000u); }
DEV u64 ldflag(const char* p) { return __hip_atomic_load((GLP(const u64))p, __ATOMIC_RELAXED, __HIP_MEMORY_SCOPE_AGENT); }
DEV void stflag(char* p, u64 v) { __hip_atomic_store((GLP(u64))p, v, __ATOMIC_RELAXED, __HIP_MEMORY_SCOPE_AGENT); }
DEV u64 mix(u64 nonce) { return nonce * 6364136223846793005ull + 1442695040888963407ull; }
DEV void poll(const char* p, u64 tag) {
#pragma unroll 1
  for (u32 i = 0; i < SPIN_MAX; i++) {
    if (__all(ldflag(p) == tag)) break;
    __builtin_amdgcn_s_sleep(2);
  }
  __builtin_amdgcn_fence(__ATOMIC_ACQUIRE, "wavefront");
}

template <int I, int N, class F> DEV void sfor(F&& f) {
  if constexpr (I < N) { f(std::integral_constant<int, I>{}); sfor<I + 1, N>(f); }
}

typedef u32 u32x8 __attribute__((ext_vector_type(8)));
typedef u32 u32x16 __attribute__((ext_vector_type(16)));
typedef u32 u32x32 __attribute__((ext_vector_type(32)));
struct Geo { int w, rb, j, head, qb, sa; u32 remoff; };
DEV u32x2 ld64(rsrc_t rs, u32 v, u32 so) { return __builtin_amdgcn_raw_buffer_load_b64(rs, v, so, SC1); }
DEV u32x4 ld128(rsrc_t rs, u32 v, u32 so) { return __builtin_amdgcn_raw_buffer_load_b128(rs, v, so, SC1); }
DEV u32x4 cat(u32x2 a, u32x2 b) { return u32x4{a[0], a[1], b[0], b[1]}; }
DEV f4 tile(const f32v& a, int t, int m) { return f4{a[8 * t + 4 * m], a[8 * t + 4 * m + 1], a[8 * t + 4 * m + 2], a[8 * t + 4 * m + 3]}; }
DEV u32x4 pair8(f4 a, f4 b) {
  u32 a0 = pk(a[0], a[1]), a1 = pk(a[2], a[3]), b0 = pk(b[0], b[1]), b1 = pk(b[2], b[3]);
  asm("s_nop 1\n\tv_permlane16_swap_b32 %0, %2\n\tv_permlane16_swap_b32 %1, %3" : "+v"(a0), "+v"(a1), "+v"(b0), "+v"(b1));
  return u32x4{a0, a1, b0, b1};
}
DEV u64 uniform64(u32 lo, u32 hi) { return (u64)(u32)__builtin_amdgcn_readfirstlane(lo) | ((u64)(u32)__builtin_amdgcn_readfirstlane(hi) << 32); }
template <int C, int M> DEV float dpp_add(float v) {
  return v + __builtin_bit_cast(float, __builtin_amdgcn_update_dpp(0, __builtin_bit_cast(int, v), C, M, 0xf, true));
}
DEV float wave_sum63(float v) {
  v = dpp_add<0x111, 0xf>(v); v = dpp_add<0x112, 0xf>(v); v = dpp_add<0x114, 0xf>(v); v = dpp_add<0x118, 0xf>(v);
  return dpp_add<0x143, 0xc>(dpp_add<0x142, 0xa>(v));
}
DEV void row_stats(u32x4 a, u32 b, char* ws, u64 tag, u32 lane, u32 row) {
  float s = dot2(b, 0x3f803f80u, 0.f), q = dot2(b, b, 0.f);
#pragma unroll
  for (int i = 0; i < 4; i++) { s = dot2(a[i], 0x3f803f80u, s); q = dot2(a[i], a[i], q); }
  const float mu = wave_sum63(s) * (1.f / 640), var = fmaxf(wave_sum63(q) * (1.f / 640) - mu * mu, 0.f);
  st128(u32x4{pk(-mu, -mu - bflo(pk(-mu, 0.f))), (u32)tag, __builtin_bit_cast(u32, var), (u32)(tag >> 32)}, mk(ws, 65536),
        lane == 63 ? F_ST + row * 16 : OOB, 0);
}

struct P1Out { f4 v[4][2]; float r[2]; u32x2 tag; u32x32 q; };
DEV u32x2 qkv_flags(u32 lane, int head, int qb) {
  const u32 jj = (head * 20 + ((lane >> 4) & 1) * 19) >> 7;
  return u32x2{F_QKV + ((5 + 5 * (lane >> 5) + jj) * 16 + (lane & 15)) * 8, F_QKV + (jj * 16 + qb * 2 + (lane & 1)) * 8};
}

DEV u32 w_frag_addr(u32 w, u32 lane) {
  const u32 g = lane >> 4, q = (lane & 15) >> 2;
  return (8 * g + q) * 256 + ((w ^ q ^ ((g & 1) << 2)) << 5) + (lane & 3) * 8;
}
DEV void late_geo(Geo& G) {
  u32 b = blockIdx.x;
  asm volatile("" : "+s"(b));
  G.head = (b & 7) * 4 + (b >> 3 & 3); G.qb = b >> 5;
  G.sa = (5 * G.head + 1) >> 1;
  G.remoff = G.head & 1 ? (G.sa - 1) * SLOT + 8 : (G.sa + 2) * SLOT;
}
DEV void phase1(const void* x, const void* wqkv, const void* w1, const void* b1, char* ws, Geo& G, u32 lane, u32 rot, P1Out& o) {
  const u32 w = G.w, rb = G.rb, j = G.j, g = lane >> 4, l15 = lane & 15, row = rb * 128 + j * 8 + w;
  const u32 vx0 = (rb * 128 + 16 * w + (lane >> 3)) * 1280 + (((lane & 7) ^ g) << 4);
  const u32 wch = l15 ^ (g << 1) ^ ((w & 1) << 3);
  u32x4 g0 = {vx0, (vx0 + 8 * 1280) ^ 64, (8 * w + g) * 3840 + j * 256 + wch * 16, 0};
  asm volatile("" ::"{v[28:31]}"(g0), "{s40}"(w * 2048), "{s41}"(rot));
  __builtin_amdgcn_sched_barrier(0);
  asm volatile(
    "s_mov_b32 s56, s41\n"
    "s_add_u32 m0, s40, 98304\n"
    "s_lshl_b32 s57, s56, 7\n"
    "buffer_load_dwordx4 v28, %[rx], s57 offen  lds\n"
    "s_add_u32 m0, s40, 99328\n"
    "s_nop 0\n"
    "buffer_load_dwordx4 v29, %[rx], s57 offen  lds\n"
    "s_add_u32 m0, s40, 114688\n"
    "s_mul_i32 s57, s56, 245760\n"
    "buffer_load_dwordx4 v30, %[rq], s57 offen  lds\n"
    "s_add_u32 m0, s40, 115712\n"
    "s_add_u32 s57, s57, 15360\n"
    "buffer_load_dwordx4 v30, %[rq], s57 offen  lds\n"
      ::"{v[28:31]}"(g0), "{s40}"(w * 2048), "{s41}"(rot), [rx] "s"(mk(x, 2048 * 1280)), [rq] "s"(mk(wqkv, 640 * 3840))
      : "s56", "s57", "m0", "scc", "memory");
  __builtin_amdgcn_sched_barrier(0);
  late_geo(G);
  const u32 ax = (32 * (w & 3) + l15) * 128 + ((g ^ (l15 >> 1)) << 4) + 65536, aw = w_frag_addr(w, lane) + 65536;
  u32x4 g1 = {ax, ax ^ 64, lane * 4, 0}, wa = {aw, aw ^ 32, aw ^ 64, aw ^ 96};
  const u32x8 sp = {w * 2048, rot, F_ST + row * 16, rb, j, (u32)G.head, (u32)G.qb, G.sa * SLOT};
  f32v acc;
  asm volatile(
    "s_mov_b32 s56, s41\n"
    "s_barrier\n"
    "buffer_load_dwordx4 v[0:3], v6, %[rx], %[srow] offen\n"
    "buffer_load_dword v4, v26, %[rx], %[srow] offen offset:1024\n"
    "buffer_load_dwordx2 v[8:9], off, %[rw], 0 sc1\n"
    "s_add_u32 s56, s56, 1\n"
    "s_cmp_eq_u32 s56, 10\n"
    "s_cselect_b32 s56, 0, s56\n"
    "s_add_u32 m0, s40, 131072\n"
    "s_lshl_b32 s57, s56, 7\n"
    "buffer_load_dwordx4 v28, %[rx], s57 offen  lds\n"
    "s_add_u32 m0, s40, 132096\n"
    "s_nop 0\n"
    "buffer_load_dwordx4 v29, %[rx], s57 offen  lds\n"
    "s_add_u32 m0, s40, 0\n"
    "s_mul_i32 s57, s56, 245760\n"
    "buffer_load_dwordx4 v30, %[rq], s57 offen  lds\n"
    "s_add_u32 m0, s40, 1024\n"
    "s_add_u32 s57, s57, 15360\n"
    "buffer_load_dwordx4 v30, %[rq], s57 offen  lds\n"
    "v_mov_b32 v36, 0x3f803f80\n"
    "v_mov_b32 v37, 0x3f803f80\n"
    "v_mov_b32 v38, 0x3f803f80\n"
    "v_mov_b32 v39, 0x3f803f80\n"
    "v_mov_b32 v10, 0\n"
    "v_mov_b32 v11, 0\n"
    "s_cmp_ge_u32 s44, 10\n"
    "s_waitcnt vmcnt(7)\n"
    "s_cbranch_scc1 .Lp1_v\n"
    "s_barrier\n"
    "ds_read_b128 v[96:99], v24 offset:32768\n"
    "ds_read_b128 v[100:103], v24 offset:34816\n"
    "ds_read_b64_tr_b16 v[44:45], v40 offset:49152\n"
    "ds_read_b64_tr_b16 v[46:47], v40 offset:50176\n"
    "ds_read_b64_tr_b16 v[48:49], v41 offset:49152\n"
    "ds_read_b64_tr_b16 v[50:51], v41 offset:50176\n"
    "ds_read_b64_tr_b16 v[52:53], v42 offset:49152\n"
    "ds_read_b64_tr_b16 v[54:55], v42 offset:50176\n"
    "ds_read_b64_tr_b16 v[56:57], v43 offset:49152\n"
    "ds_read_b64_tr_b16 v[58:59], v43 offset:50176\n"
    "s_waitcnt lgkmcnt(6)\n"
    "v_mfma_f32_16x16x32_bf16 v[32:35], v[36:39], v[44:47], 0\n"
    "v_mfma_f32_16x16x32_bf16 v[64:67], v[44:47], v[96:99], 0\n"
    "s_setprio 1\n"
    "ds_read_b128 v[104:107], v25 offset:32768\n"
    "ds_read_b128 v[108:111], v25 offset:34816\n"
    "ds_read_b64_tr_b16 v[60:61], v40 offset:57344\n"
    "ds_read_b64_tr_b16 v[62:63], v40 offset:58368\n"
    "s_and_b32 s64, s43, 15\n"
    "s_lshr_b32 s65, s40, 11\n"
    "v_mfma_f32_16x16x32_bf16 v[68:71], v[44:47], v[100:103], 0\n"
    "v_lshrrev_b32 v255, 5, v6\n"
    "s_lshl3_add_u32 s64, s64, s65\n"
    "s_add_u32 s56, s56, 1\n"
    "s_cmp_eq_u32 s56, 10\n"
    "s_cselect_b32 s56, 0, s56\n"
    "s_add_u32 m0, s40, 16384\n"
    "s_lshl_b32 s57, s56, 7\n"
    "buffer_load_dwordx4 v28, %[rx], s57 offen  lds\n"
    "s_waitcnt lgkmcnt(8)\n"
    "v_mfma_f32_16x16x32_bf16 v[72:75], v[48:51], v[96:99], 0\n"
    "ds_read_b64_tr_b16 v[44:45], v41 offset:57344\n"
    "ds_read_b64_tr_b16 v[46:47], v41 offset:58368\n"
    "v_mul_u32_u24 v255, 3456, v255\n"
    "s_mul_i32 s64, s64, 17280\n"
    "v_mfma_f32_16x16x32_bf16 v[76:79], v[48:51], v[100:103], 0\n"
    "v_and_b32 v252, 16, v6\n"
    "s_lshl_b32 s65, s44, 8\n"
    "s_add_u32 m0, s40, 17408\n"
    "s_nop 0\n"
    "buffer_load_dwordx4 v29, %[rx], s57 offen  lds\n"
    "s_waitcnt lgkmcnt(8)\n"
    "v_mfma_f32_16x16x32_bf16 v[80:83], v[52:55], v[96:99], 0\n"
    "s_setprio 0\n"
    "ds_read_b64_tr_b16 v[48:49], v42 offset:57344\n"
    "ds_read_b64_tr_b16 v[50:51], v42 offset:58368\n"
    "v_lshl_add_u32 v255, v252, 3, v255\n"
    "s_add_u32 s64, s64, s65\n"
    "v_mfma_f32_16x16x32_bf16 v[84:87], v[52:55], v[100:103], 0\n"
    "v_add_u32 v255, s64, v255\n"
    "s_mov_b64 exec, 1023\nbuffer_load_dword v255, v255, %[r1], 0 offen\ns_mov_b64 exec, -1\n"
    "s_add_u32 m0, s40, 32768\n"
    "s_mul_i32 s57, s56, 245760\n"
    "buffer_load_dwordx4 v30, %[rq], s57 offen  lds\n"
    "s_waitcnt lgkmcnt(8)\n"
    "v_mfma_f32_16x16x32_bf16 v[88:91], v[56:59], v[96:99], 0\n"
    "ds_read_b64_tr_b16 v[52:53], v43 offset:57344\n"
    "ds_read_b64_tr_b16 v[54:55], v43 offset:58368\n"
    "v_mfma_f32_16x16x32_bf16 v[92:95], v[56:59], v[100:103], 0\n"
    "s_add_u32 m0, s40, 33792\n"
    "s_add_u32 s57, s57, 15360\n"
    "buffer_load_dwordx4 v30, %[rq], s57 offen  lds\n"
    "s_waitcnt lgkmcnt(6)\n"
    "v_mfma_f32_16x16x32_bf16 v[32:35], v[36:39], v[60:63], v[32:35]\n"
    "v_mfma_f32_16x16x32_bf16 v[64:67], v[60:63], v[104:107], v[64:67]\n"
    "s_waitcnt vmcnt(5)\n"
    "s_barrier\n"
    "s_setprio 3\n"
    "v_add_u32 v24, 0x10000, v24\n"
    "v_add_u32 v25, 0x10000, v25\n"
    "ds_read_b128 v[112:115], v24 offset:0\n"
    "ds_read_b128 v[116:119], v24 offset:2048\n"
    "v_add_u32 v40, 0xffff0000, v40\n"
    "v_add_u32 v41, 0xffff0000, v41\n"
    "v_add_u32 v42, 0xffff0000, v42\n"
    "v_add_u32 v43, 0xffff0000, v43\n"
    "ds_read_b64_tr_b16 v[56:57], v40 offset:0\n"
    "ds_read_b64_tr_b16 v[58:59], v40 offset:1024\n"
    "v_mfma_f32_16x16x32_bf16 v[68:71], v[60:63], v[108:111], v[68:71]\n"
    "s_add_u32 s56, s56, 1\n"
    "s_cmp_eq_u32 s56, 10\n"
    "s_cselect_b32 s56, 0, s56\n"
    "s_add_u32 m0, s40, 49152\n"
    "s_lshl_b32 s57, s56, 7\n"
    "buffer_load_dwordx4 v28, %[rx], s57 offen  lds\n"
    "s_waitcnt lgkmcnt(8)\n"
    "v_mfma_f32_16x16x32_bf16 v[72:75], v[44:47], v[104:107], v[72:75]\n"
    "ds_read_b64_tr_b16 v[60:61], v41 offset:0\n"
    "ds_read_b64_tr_b16 v[62:63], v41 offset:1024\n"
    "v_readfirstlane_b32 s60, v8\n"
    "v_readfirstlane_b32 s61, v9\n"
    "v_mfma_f32_16x16x32_bf16 v[76:79], v[44:47], v[108:111], v[76:79]\n"
    "v_dot2c_f32_bf16 v10, v36, v0\n"
    "v_dot2c_f32_bf16 v11, v0, v0\n"
    "s_add_u32 m0, s40, 50176\n"
    "s_nop 0\n"
    "buffer_load_dwordx4 v29, %[rx], s57 offen  lds\n"
    "s_waitcnt lgkmcnt(8)\n"
    "v_mfma_f32_16x16x32_bf16 v[80:83], v[48:51], v[104:107], v[80:83]\n"
    "s_setprio 2\n"
    "ds_read_b64_tr_b16 v[44:45], v42 offset:0\n"
    "ds_read_b64_tr_b16 v[46:47], v42 offset:1024\n"
    "v_dot2c_f32_bf16 v10, v36, v1\n"
    "v_dot2c_f32_bf16 v11, v1, v1\n"
    "v_mfma_f32_16x16x32_bf16 v[84:87], v[48:51], v[108:111], v[84:87]\n"
    "v_dot2c_f32_bf16 v10, v36, v2\n"
    "v_dot2c_f32_bf16 v11, v2, v2\n"
    "s_waitcnt lgkmcnt(8)\n"
    "v_mfma_f32_16x16x32_bf16 v[88:91], v[52:55], v[104:107], v[88:91]\n"
    "ds_read_b64_tr_b16 v[48:49], v43 offset:0\n"
    "ds_read_b64_tr_b16 v[50:51], v43 offset:1024\n"
    "v_dot2c_f32_bf16 v10, v36, v3\n"
    "v_dot2c_f32_bf16 v11, v3, v3\n"
    "v_mfma_f32_16x16x32_bf16 v[92:95], v[52:55], v[108:111], v[92:95]\n"
    "v_dot2c_f32_bf16 v10, v36, v4\n"
    "v_dot2c_f32_bf16 v11, v4, v4\n"
    "s_add_u32 m0, s40, 65536\n"
    "s_mul_i32 s57, s56, 245760\n"
    "buffer_load_dwordx4 v30, %[rq], s57 offen  lds\n"
    "s_waitcnt lgkmcnt(6)\n"
    "v_mfma_f32_16x16x32_bf16 v[32:35], v[36:39], v[56:59], v[32:35]\n"
    "v_mfma_f32_16x16x32_bf16 v[64:67], v[56:59], v[112:115], v[64:67]\n"
    "s_setprio 1\n"
    "ds_read_b128 v[120:123], v25 offset:0\n"
    "ds_read_b128 v[124:127], v25 offset:2048\n"
    "ds_read_b64_tr_b16 v[52:53], v40 offset:8192\n"
    "ds_read_b64_tr_b16 v[54:55], v40 offset:9216\n"
    "s_mul_i32 s61, s61, 0x4c957f2d\n"
    "s_mul_i32 s58, s60, 0x5851f42d\n"
    "v_mfma_f32_16x16x32_bf16 v[68:71], v[56:59], v[116:119], v[68:71]\n"
    "s_add_u32 s61, s61, s58\n"
    "v_add_f32_dpp v10, v10, v10 row_shr:1 row_mask:0xf bank_mask:0xf bound_ctrl:1\n"
    "s_waitcnt lgkmcnt(8)\n"
    "v_mfma_f32_16x16x32_bf16 v[72:75], v[60:63], v[112:115], v[72:75]\n"
    "ds_read_b64_tr_b16 v[56:57], v41 offset:8192\n"
    "ds_read_b64_tr_b16 v[58:59], v41 offset:9216\n"
    "v_add_f32_dpp v11, v11, v11 row_shr:1 row_mask:0xf bank_mask:0xf bound_ctrl:1\n"
    "s_mul_hi_u32 s58, s60, 0x4c957f2d\n"
    "v_mfma_f32_16x16x32_bf16 v[76:79], v[60:63], v[116:119], v[76:79]\n"
    "v_add_f32_dpp v10, v10, v10 row_shr:2 row_mask:0xf bank_mask:0xf bound_ctrl:1\n"
    "v_add_f32_dpp v11, v11, v11 row_shr:2 row_mask:0xf bank_mask:0xf bound_ctrl:1\n"
    "s_add_u32 m0, s40, 66560\n"
    "s_add_u32 s57, s57, 15360\n"
    "buffer_load_dwordx4 v30, %[rq], s57 offen  lds\n"
    "s_waitcnt lgkmcnt(8)\n"
    "v_mfma_f32_16x16x32_bf16 v[80:83], v[44:47], v[112:115], v[80:83]\n"
    "s_setprio 0\n"
    "ds_read_b64_tr_b16 v[60:61], v42 offset:8192\n"
    "ds_read_b64_tr_b16 v[62:63], v42 offset:9216\n"
    "s_add_u32 s61, s61, s58\n"
    "v_add_f32_dpp v10, v10, v10 row_shr:4 row_mask:0xf bank_mask:0xf bound_ctrl:1\n"
    "v_mfma_f32_16x16x32_bf16 v[84:87], v[44:47], v[116:119], v[84:87]\n"
    "v_add_f32_dpp v11, v11, v11 row_shr:4 row_mask:0xf bank_mask:0xf bound_ctrl:1\n"
    "s_mul_i32 s60, s60, 0x4c957f2d\n"
    "s_waitcnt lgkmcnt(8)\n"
    "v_mfma_f32_16x16x32_bf16 v[88:91], v[48:51], v[112:115], v[88:91]\n"
    "ds_read_b64_tr_b16 v[44:45], v43 offset:8192\n"
    "ds_read_b64_tr_b16 v[46:47], v43 offset:9216\n"
    "v_add_f32_dpp v10, v10, v10 row_shr:8 row_mask:0xf bank_mask:0xf bound_ctrl:1\n"
    "v_add_f32_dpp v11, v11, v11 row_shr:8 row_mask:0xf bank_mask:0xf bound_ctrl:1\n"
    "v_mfma_f32_16x16x32_bf16 v[92:95], v[48:51], v[116:119], v[92:95]\n"
    "s_add_u32 s60, s60, 0xf767814f\ns_addc_u32 s61, s61, 0x14057b7e\n"
    "v_add_f32_dpp v10, v10, v10 row_bcast:15 row_mask:0xa bank_mask:0xf bound_ctrl:1\n"
    "s_add_u32 s56, s56, 1\n"
    "s_cmp_eq_u32 s56, 10\n"
    "s_cselect_b32 s56, 0, s56\n"
    "s_add_u32 m0, s40, 81920\n"
    "s_lshl_b32 s57, s56, 7\n"
    "buffer_load_dwordx4 v28, %[rx], s57 offen  lds\n"
    "s_waitcnt lgkmcnt(6)\n"
    "v_mfma_f32_16x16x32_bf16 v[32:35], v[36:39], v[52:55], v[32:35]\n"
    "v_mfma_f32_16x16x32_bf16 v[64:67], v[52:55], v[120:123], v[64:67]\n"
    "s_waitcnt vmcnt(5)\n"
    "s_barrier\n"
    "s_setprio 3\n"
    "v_add_u32 v24, 0xfffe0000, v24\n"
    "v_add_u32 v25, 0xfffe0000, v25\n"
    "ds_read_b128 v[128:131], v24 offset:16384\n"
    "ds_read_b128 v[132:135], v24 offset:18432\n"
    "ds_read_b64_tr_b16 v[48:49], v40 offset:32768\n"
    "ds_read_b64_tr_b16 v[50:51], v40 offset:33792\n"
    "v_add_f32_dpp v11, v11, v11 row_bcast:15 row_mask:0xa bank_mask:0xf bound_ctrl:1\n"
    "v_mov_b32 v13, s60\n"
    "v_mfma_f32_16x16x32_bf16 v[68:71], v[52:55], v[124:127], v[68:71]\n"
    "v_add_f32_dpp v10, v10, v10 row_bcast:31 row_mask:0xc bank_mask:0xf bound_ctrl:1\n"
    "v_add_f32_dpp v11, v11, v11 row_bcast:31 row_mask:0xc bank_mask:0xf bound_ctrl:1\n"
    "s_add_u32 m0, s40, 82944\n"
    "s_nop 0\n"
    "buffer_load_dwordx4 v29, %[rx], s57 offen  lds\n"
    "s_waitcnt lgkmcnt(8)\n"
    "v_mfma_f32_16x16x32_bf16 v[72:75], v[56:59], v[120:123], v[72:75]\n"
    "ds_read_b64_tr_b16 v[52:53], v41 offset:32768\n"
    "ds_read_b64_tr_b16 v[54:55], v41 offset:33792\n"
    "v_mov_b32 v15, s61\n"
    "v_mul_f32 v10, 0xbacccccd, v10\n"
    "v_mfma_f32_16x16x32_bf16 v[76:79], v[56:59], v[124:127], v[76:79]\n"
    "v_mul_f32 v11, 0x3acccccd, v11\n"
    "v_cvt_pk_bf16_f32 v12, v10, v10\n"
    "s_add_u32 m0, s40, 98304\n"
    "s_mul_i32 s57, s56, 245760\n"
    "buffer_load_dwordx4 v30, %[rq], s57 offen  lds\n"
    "s_waitcnt lgkmcnt(8)\n"
    "v_mfma_f32_16x16x32_bf16 v[80:83], v[60:63], v[120:123], v[80:83]\n"
    "s_setprio 2\n"
    "ds_read_b64_tr_b16 v[56:57], v42 offset:32768\n"
    "ds_read_b64_tr_b16 v[58:59], v42 offset:33792\n"
    "v_fma_f32 v11, -v10, v10, v11\n"
    "v_lshlrev_b32 v12, 16, v12\n"
    "v_mfma_f32_16x16x32_bf16 v[84:87], v[60:63], v[124:127], v[84:87]\n"
    "v_max_f32 v14, 0, v11\n"
    "v_sub_f32 v12, v10, v12\n"
    "s_waitcnt lgkmcnt(8)\n"
    "v_mfma_f32_16x16x32_bf16 v[88:91], v[44:47], v[120:123], v[88:91]\n"
    "ds_read_b64_tr_b16 v[60:61], v43 offset:32768\n"
    "ds_read_b64_tr_b16 v[62:63], v43 offset:33792\n"
    "v_cvt_pk_bf16_f32 v12, v10, v12\n"
    "s_lshl_b64 exec, 1, 63\nbuffer_store_dwordx4 v[12:15], off, %[rw], s42 sc1\ns_mov_b64 exec, -1\ns_nop 0\n"
    "v_mfma_f32_16x16x32_bf16 v[92:95], v[44:47], v[124:127], v[92:95]\n"
    "v_lshrrev_b32 v16, 8, v6\n"
    "v_bfe_u32 v17, v6, 4, 4\n"
    "s_add_u32 m0, s40, 99328\n"
    "s_add_u32 s57, s57, 15360\n"
    "buffer_load_dwordx4 v30, %[rq], s57 offen  lds\n"
    "s_waitcnt lgkmcnt(6)\n"
    "v_mfma_f32_16x16x32_bf16 v[32:35], v[36:39], v[48:51], v[32:35]\n"
    "v_mfma_f32_16x16x32_bf16 v[64:67], v[48:51], v[128:131], v[64:67]\n"
    "s_setprio 1\n"
    "ds_read_b128 v[136:139], v25 offset:16384\n"
    "ds_read_b128 v[140:143], v25 offset:18432\n"
    "ds_read_b64_tr_b16 v[44:45], v40 offset:40960\n"
    "ds_read_b64_tr_b16 v[46:47], v40 offset:41984\n"
    "s_movk_i32 s59, 3456\n"
    "s_mov_b32 s55, 221184\n"
    "v_mfma_f32_16x16x32_bf16 v[68:71], v[48:51], v[132:135], v[68:71]\n"
    "s_lshl_b32 s64, s44, 8\n"
    "s_mul_i32 s65, s43, 81920\n"
    "s_waitcnt lgkmcnt(8)\n"
    "v_mfma_f32_16x16x32_bf16 v[72:75], v[52:55], v[128:131], v[72:75]\n"
    "ds_read_b64_tr_b16 v[48:49], v41 offset:40960\n"
    "ds_read_b64_tr_b16 v[50:51], v41 offset:41984\n"
    "s_cmp_eq_u32 s44, 14\ns_cmovk_i32 s59, 1280\ns_cmovk_i32 s55, 256\ns_cselect_b32 s64, s65, s64\n"
    "s_lshr_b32 s65, s40, 8\n"
    "v_mfma_f32_16x16x32_bf16 v[76:79], v[52:55], v[132:135], v[76:79]\n"
    "s_mul_i32 s65, s65, s59\n"
    "v_and_b32 v18, 0xf0, v30\n"
    "s_add_u32 s56, s56, 1\n"
    "s_cmp_eq_u32 s56, 10\n"
    "s_cselect_b32 s56, 0, s56\n"
    "s_add_u32 m0, s40, 114688\n"
    "s_lshl_b32 s57, s56, 7\n"
    "buffer_load_dwordx4 v28, %[rx], s57 offen  lds\n"
    "s_waitcnt lgkmcnt(8)\n"
    "v_mfma_f32_16x16x32_bf16 v[80:83], v[56:59], v[128:131], v[80:83]\n"
    "s_setprio 0\n"
    "ds_read_b64_tr_b16 v[52:53], v42 offset:40960\n"
    "ds_read_b64_tr_b16 v[54:55], v42 offset:41984\n"
    "s_add_u32 s64, s64, s65\n"
    "v_mad_u32_u24 v23, v16, s59, v18\n"
    "v_mfma_f32_16x16x32_bf16 v[84:87], v[56:59], v[132:135], v[84:87]\n"
    "s_lshl_b32 s59, s59, 2\n"
    "v_add_u32 v23, s64, v23\n"
    "s_waitcnt lgkmcnt(8)\n"
    "v_mfma_f32_16x16x32_bf16 v[88:91], v[60:63], v[128:131], v[88:91]\n"
    "ds_read_b64_tr_b16 v[56:57], v43 offset:40960\n"
    "ds_read_b64_tr_b16 v[58:59], v43 offset:41984\n"
    "s_lshr_b32 s64, s40, 2\n"
    "v_and_b32 v26, 0x1f0, v6\n"
    "v_mfma_f32_16x16x32_bf16 v[92:95], v[60:63], v[132:135], v[92:95]\n"
    "s_and_b32 s64, s64, 0x600\n"
    "v_and_b32 v18, 0x200, v6\n"
    "s_add_u32 m0, s40, 115712\n"
    "s_nop 0\n"
    "buffer_load_dwordx4 v29, %[rx], s57 offen  lds\n"
    "s_waitcnt lgkmcnt(6)\n"
    "v_mfma_f32_16x16x32_bf16 v[32:35], v[36:39], v[44:47], v[32:35]\n"
    "v_mfma_f32_16x16x32_bf16 v[64:67], v[44:47], v[136:139], v[64:67]\n"
    "s_waitcnt vmcnt(7)\n"
    "s_barrier\n"
    "s_setprio 3\n"
    "ds_read_b128 v[144:147], v24 offset:49152\n"
    "ds_read_b128 v[148:151], v24 offset:51200\n"
    "v_add_u32 v40, 0x10000, v40\n"
    "v_add_u32 v41, 0x10000, v41\n"
    "v_add_u32 v42, 0x10000, v42\n"
    "v_add_u32 v43, 0x10000, v43\n"
    "ds_read_b64_tr_b16 v[60:61], v40 offset:0\n"
    "ds_read_b64_tr_b16 v[62:63], v40 offset:1024\n"
    "s_lshl_b32 s65, s43, 11\n"
    "v_lshl_or_b32 v5, v18, 6, v26\n"
    "v_mfma_f32_16x16x32_bf16 v[68:71], v[44:47], v[140:143], v[68:71]\n"
    "s_add_u32 s64, s64, s65\n"
    "s_add_u32 s52, s64, 32768\n"
    "s_waitcnt lgkmcnt(8)\n"
    "v_mfma_f32_16x16x32_bf16 v[72:75], v[48:51], v[136:139], v[72:75]\n"
    "ds_read_b64_tr_b16 v[44:45], v41 offset:0\n"
    "ds_read_b64_tr_b16 v[46:47], v41 offset:1024\n"
    "s_lshl_b32 s65, s44, 19\n"
    "s_add_u32 s65, s65, s64\n"
    "v_mfma_f32_16x16x32_bf16 v[76:79], v[48:51], v[140:143], v[76:79]\n"
    "s_add_u32 s50, s65, 65536\n"
    "s_cmp_lt_u32 s44, 5\ns_cselect_b32 s49, 0x3ea52b5b, 1.0\n"
    "s_add_u32 m0, s40, 131072\n"
    "s_mul_i32 s57, s56, 245760\n"
    "buffer_load_dwordx4 v30, %[rq], s57 offen  lds\n"
    "s_waitcnt lgkmcnt(8)\n"
    "v_mfma_f32_16x16x32_bf16 v[80:83], v[52:55], v[136:139], v[80:83]\n"
    "s_setprio 2\n"
    "ds_read_b64_tr_b16 v[48:49], v42 offset:0\n"
    "ds_read_b64_tr_b16 v[50:51], v42 offset:1024\n"
    "s_lshl_b32 s64, s44, 7\n"
    "s_lshl3_add_u32 s64, s43, s64\n"
    "v_mfma_f32_16x16x32_bf16 v[84:87], v[52:55], v[140:143], v[84:87]\n"
    "s_add_u32 s51, s64, 1024\n"
    "s_lshr_b32 s64, s40, 5\n"
    "s_waitcnt lgkmcnt(8)\n"
    "v_mfma_f32_16x16x32_bf16 v[88:91], v[56:59], v[136:139], v[88:91]\n"
    "ds_read_b64_tr_b16 v[52:53], v43 offset:0\n"
    "ds_read_b64_tr_b16 v[54:55], v43 offset:1024\n"
    "s_add_u32 s64, s64, 162816\n"
    "v_lshl_add_u32 v21, v17, 2, s64\n"
    "v_mfma_f32_16x16x32_bf16 v[92:95], v[56:59], v[140:143], v[92:95]\n"
    "s_lshr_b32 s64, s40, 6\n"
    "s_lshl_b32 s65, s44, 8\n"
    "s_add_u32 m0, s40, 132096\n"
    "s_add_u32 s57, s57, 15360\n"
    "buffer_load_dwordx4 v30, %[rq], s57 offen  lds\n"
    "s_waitcnt lgkmcnt(6)\n"
    "v_mfma_f32_16x16x32_bf16 v[32:35], v[36:39], v[60:63], v[32:35]\n"
    "v_mfma_f32_16x16x32_bf16 v[64:67], v[60:63], v[144:147], v[64:67]\n"
    "s_setprio 1\n"
    "ds_read_b128 v[152:155], v25 offset:49152\n"
    "ds_read_b128 v[156:159], v25 offset:51200\n"
    "ds_read_b64_tr_b16 v[56:57], v40 offset:8192\n"
    "ds_read_b64_tr_b16 v[58:59], v40 offset:9216\n"
    "s_add_u32 s64, s64, s65\n"
    "v_lshl_add_u32 v7, v17, 1, s64\n"
    "v_mfma_f32_16x16x32_bf16 v[68:71], v[60:63], v[148:151], v[68:71]\n"
    "s_mov_b32 s64, 0x02020202\n"
    "v_mul_lo_u32 v22, v16, s64\n"
    "s_waitcnt lgkmcnt(8)\n"
    "v_mfma_f32_16x16x32_bf16 v[72:75], v[44:47], v[144:147], v[72:75]\n"
    "ds_read_b64_tr_b16 v[60:61], v41 offset:8192\n"
    "ds_read_b64_tr_b16 v[62:63], v41 offset:9216\n"
    "s_mul_i32 s64, s45, 20\n"
    "v_add_u32 v22, 0x01000100, v22\n"
    "v_mfma_f32_16x16x32_bf16 v[76:79], v[44:47], v[148:151], v[76:79]\n"
    "s_add_u32 s65, s64, 19\n"
    "v_lshrrev_b32 v18, 1, v6\n"
    "s_add_u32 s56, s56, 1\n"
    "s_cmp_eq_u32 s56, 10\n"
    "s_cselect_b32 s56, 0, s56\n"
    "s_add_u32 m0, s40, 0\n"
    "s_lshl_b32 s57, s56, 7\n"
    "buffer_load_dwordx4 v28, %[rx], s57 offen  lds\n"
    "s_waitcnt lgkmcnt(8)\n"
    "v_mfma_f32_16x16x32_bf16 v[80:83], v[48:51], v[144:147], v[80:83]\n"
    "s_setprio 0\n"
    "ds_read_b64_tr_b16 v[44:45], v42 offset:8192\n"
    "ds_read_b64_tr_b16 v[46:47], v42 offset:9216\n"
    "s_xor_b32 s65, s65, s64\n"
    "v_lshrrev_b32 v19, 9, v6\n"
    "v_mfma_f32_16x16x32_bf16 v[84:87], v[48:51], v[148:151], v[84:87]\n"
    "s_and_b32 s65, s65, 0x80\n"
    "v_mul_u32_u24 v19, 640, v19\n"
    "s_waitcnt lgkmcnt(8)\n"
    "v_mfma_f32_16x16x32_bf16 v[88:91], v[52:55], v[144:147], v[88:91]\n"
    "ds_read_b64_tr_b16 v[48:49], v43 offset:8192\n"
    "ds_read_b64_tr_b16 v[50:51], v43 offset:9216\n"
    "s_or_b32 s65, s65, 0x7f\n"
    "v_and_b32 v18, s65, v18\n"
    "v_mfma_f32_16x16x32_bf16 v[92:95], v[52:55], v[148:151], v[92:95]\n"
    "s_and_b32 s64, s64, 0xffffff80\n"
    "v_and_b32 v13, 0x88, v18\n"
    "s_add_u32 m0, s40, 1024\n"
    "s_nop 0\n"
    "buffer_load_dwordx4 v29, %[rx], s57 offen  lds\n"
    "s_waitcnt lgkmcnt(6)\n"
    "v_mfma_f32_16x16x32_bf16 v[32:35], v[36:39], v[56:59], v[32:35]\n"
    "v_mfma_f32_16x16x32_bf16 v[64:67], v[56:59], v[152:155], v[64:67]\n"
    "s_waitcnt vmcnt(6)\n"
    "s_barrier\n"
    "s_setprio 3\n"
    "v_add_u32 v24, 0x10000, v24\n"
    "v_add_u32 v25, 0x10000, v25\n"
    "ds_read_b128 v[160:163], v24 offset:16384\n"
    "ds_read_b128 v[164:167], v24 offset:18432\n"
    "ds_read_b64_tr_b16 v[52:53], v40 offset:32768\n"
    "ds_read_b64_tr_b16 v[54:55], v40 offset:33792\n"
    "s_add_u32 s53, s64, 1664\n"
    "v_add_u32 v12, v18, v19\n"
    "v_mfma_f32_16x16x32_bf16 v[68:71], v[56:59], v[156:159], v[68:71]\n"
    "s_lshl4_add_u32 s64, s46, s64\n"
    "s_add_u32 s54, s64, 1024\n"
    "s_waitcnt lgkmcnt(8)\n"
    "v_mfma_f32_16x16x32_bf16 v[72:75], v[60:63], v[152:155], v[72:75]\n"
    "ds_read_b64_tr_b16 v[56:57], v41 offset:32768\n"
    "ds_read_b64_tr_b16 v[58:59], v41 offset:33792\n"
    "v_and_b32 v18, 0xfffffeff, v5\n"
    "s_lshr_b32 s64, s40, 11\n"
    "v_mfma_f32_16x16x32_bf16 v[76:79], v[60:63], v[156:159], v[76:79]\n"
    "v_and_b32 v19, 1, v16\n"
    "v_xor_b32 v19, s64, v19\n"
    "s_add_u32 m0, s40, 16384\n"
    "s_mul_i32 s57, s56, 245760\n"
    "buffer_load_dwordx4 v30, %[rq], s57 offen  lds\n"
    "s_waitcnt lgkmcnt(8)\n"
    "v_mfma_f32_16x16x32_bf16 v[80:83], v[44:47], v[152:155], v[80:83]\n"
    "s_setprio 2\n"
    "ds_read_b64_tr_b16 v[60:61], v42 offset:32768\n"
    "ds_read_b64_tr_b16 v[62:63], v42 offset:33792\n"
    "v_lshl_or_b32 v0, v19, 16, v18\n"
    "v_xor_b32 v1, 0x100, v0\n"
    "v_mfma_f32_16x16x32_bf16 v[84:87], v[44:47], v[156:159], v[84:87]\n"
    "v_xor_b32 v2, 0x20000, v0\n"
    "v_xor_b32 v3, 0x20100, v0\n"
    "s_waitcnt lgkmcnt(8)\n"
    "v_mfma_f32_16x16x32_bf16 v[88:91], v[48:51], v[152:155], v[88:91]\n"
    "ds_read_b64_tr_b16 v[44:45], v43 offset:32768\n"
    "ds_read_b64_tr_b16 v[46:47], v43 offset:33792\n"
    "s_lshl4_add_u32 s64, s44, s64\n"
    "v_mov_b32 v16, -1\n"
    "v_mfma_f32_16x16x32_bf16 v[92:95], v[48:51], v[156:159], v[92:95]\n"
    "s_cmp_ge_u32 s44, 5\ns_cselect_b32 s65, 72, 0\n"
    "v_mov_b32 v17, -1\n"
    "s_add_u32 m0, s40, 17408\n"
    "s_add_u32 s57, s57, 15360\n"
    "buffer_load_dwordx4 v30, %[rq], s57 offen  lds\n"
    "s_waitcnt lgkmcnt(6)\n"
    "v_mfma_f32_16x16x32_bf16 v[32:35], v[36:39], v[52:55], v[32:35]\n"
    "v_mfma_f32_16x16x32_bf16 v[64:67], v[52:55], v[160:163], v[64:67]\n"
    "s_setprio 1\n"
    "ds_read_b128 v[168:171], v25 offset:16384\n"
    "ds_read_b128 v[172:175], v25 offset:18432\n"
    "ds_read_b64_tr_b16 v[48:49], v40 offset:40960\n"
    "ds_read_b64_tr_b16 v[50:51], v40 offset:41984\n"
    "s_sub_u32 s64, s64, s65\n"
    "v_mov_b32 v18, -1\n"
    "v_mfma_f32_16x16x32_bf16 v[68:71], v[52:55], v[164:167], v[68:71]\n"
    "s_lshl4_add_u32 s64, s64, s43\n"
    "v_mov_b32 v19, -1\n"
    "s_waitcnt lgkmcnt(8)\n"
    "v_mfma_f32_16x16x32_bf16 v[72:75], v[56:59], v[160:163], v[72:75]\n"
    "ds_read_b64_tr_b16 v[52:53], v41 offset:40960\n"
    "ds_read_b64_tr_b16 v[54:55], v41 offset:41984\n"
    "s_lshl_b32 s64, s64, 11\n"
    "s_add_u32 s64, s64, 18071552\n"
    "v_mfma_f32_16x16x32_bf16 v[76:79], v[56:59], v[164:167], v[76:79]\n"
    "buffer_store_dwordx4 v[16:19], v6, %[rw], s64 offen offset:0 sc1\n"
    "buffer_store_dwordx4 v[16:19], v6, %[rw], s64 offen offset:1024 sc1\n"
    "s_add_u32 s56, s56, 1\n"
    "s_cmp_eq_u32 s56, 10\n"
    "s_cselect_b32 s56, 0, s56\n"
    "s_add_u32 m0, s40, 32768\n"
    "s_lshl_b32 s57, s56, 7\n"
    "buffer_load_dwordx4 v28, %[rx], s57 offen  lds\n"
    "s_waitcnt lgkmcnt(8)\n"
    "v_mfma_f32_16x16x32_bf16 v[80:83], v[60:63], v[160:163], v[80:83]\n"
    "s_setprio 0\n"
    "ds_read_b64_tr_b16 v[56:57], v42 offset:40960\n"
    "ds_read_b64_tr_b16 v[58:59], v42 offset:41984\n"
    "v_mfma_f32_16x16x32_bf16 v[84:87], v[60:63], v[164:167], v[84:87]\n"
    "s_waitcnt lgkmcnt(8)\n"
    "v_mfma_f32_16x16x32_bf16 v[88:91], v[44:47], v[160:163], v[88:91]\n"
    "ds_read_b64_tr_b16 v[60:61], v43 offset:40960\n"
    "ds_read_b64_tr_b16 v[62:63], v43 offset:41984\n"
    "v_mfma_f32_16x16x32_bf16 v[92:95], v[44:47], v[164:167], v[92:95]\n"
    "s_add_u32 m0, s40, 33792\n"
    "s_nop 0\n"
    "buffer_load_dwordx4 v29, %[rx], s57 offen  lds\n"
    "s_waitcnt lgkmcnt(6)\n"
    "v_mfma_f32_16x16x32_bf16 v[32:35], v[36:39], v[48:51], v[32:35]\n"
    "v_mfma_f32_16x16x32_bf16 v[64:67], v[48:51], v[168:171], v[64:67]\n"
    "s_waitcnt vmcnt(8)\n"
    "s_barrier\n"
    "s_setprio 3\n"
    "ds_read_b128 v[176:179], v24 offset:49152\n"
    "ds_read_b128 v[180:183], v24 offset:51200\n"
    "v_add_u32 v40, 0x10000, v40\n"
    "v_add_u32 v41, 0x10000, v41\n"
    "v_add_u32 v42, 0x10000, v42\n"
    "v_add_u32 v43, 0x10000, v43\n"
    "ds_read_b64_tr_b16 v[44:45], v40 offset:0\n"
    "ds_read_b64_tr_b16 v[46:47], v40 offset:1024\n"
    "v_mfma_f32_16x16x32_bf16 v[68:71], v[48:51], v[172:175], v[68:71]\n"
    "s_waitcnt lgkmcnt(8)\n"
    "v_mfma_f32_16x16x32_bf16 v[72:75], v[52:55], v[168:171], v[72:75]\n"
    "ds_read_b64_tr_b16 v[48:49], v41 offset:0\n"
    "ds_read_b64_tr_b16 v[50:51], v41 offset:1024\n"
    "v_mfma_f32_16x16x32_bf16 v[76:79], v[52:55], v[172:175], v[76:79]\n"
    "s_add_u32 m0, s40, 49152\n"
    "s_mul_i32 s57, s56, 245760\n"
    "buffer_load_dwordx4 v30, %[rq], s57 offen  lds\n"
    "s_waitcnt lgkmcnt(8)\n"
    "v_mfma_f32_16x16x32_bf16 v[80:83], v[56:59], v[168:171], v[80:83]\n"
    "s_setprio 2\n"
    "ds_read_b64_tr_b16 v[52:53], v42 offset:0\n"
    "ds_read_b64_tr_b16 v[54:55], v42 offset:1024\n"
    "v_mfma_f32_16x16x32_bf16 v[84:87], v[56:59], v[172:175], v[84:87]\n"
    "s_waitcnt lgkmcnt(8)\n"
    "v_mfma_f32_16x16x32_bf16 v[88:91], v[60:63], v[168:171], v[88:91]\n"
    "ds_read_b64_tr_b16 v[56:57], v43 offset:0\n"
    "ds_read_b64_tr_b16 v[58:59], v43 offset:1024\n"
    "v_mfma_f32_16x16x32_bf16 v[92:95], v[60:63], v[172:175], v[92:95]\n"
    "s_add_u32 m0, s40, 50176\n"
    "s_add_u32 s57, s57, 15360\n"
    "buffer_load_dwordx4 v30, %[rq], s57 offen  lds\n"
    "s_waitcnt lgkmcnt(6)\n"
    "v_mfma_f32_16x16x32_bf16 v[32:35], v[36:39], v[44:47], v[32:35]\n"
    "v_mfma_f32_16x16x32_bf16 v[64:67], v[44:47], v[176:179], v[64:67]\n"
    "s_setprio 1\n"
    "ds_read_b128 v[184:187], v25 offset:49152\n"
    "ds_read_b128 v[188:191], v25 offset:51200\n"
    "ds_read_b64_tr_b16 v[60:61], v40 offset:8192\n"
    "ds_read_b64_tr_b16 v[62:63], v40 offset:9216\n"
    "v_mfma_f32_16x16x32_bf16 v[68:71], v[44:47], v[180:183], v[68:71]\n"
    "s_waitcnt lgkmcnt(8)\n"
    "v_mfma_f32_16x16x32_bf16 v[72:75], v[48:51], v[176:179], v[72:75]\n"
    "ds_read_b64_tr_b16 v[44:45], v41 offset:8192\n"
    "ds_read_b64_tr_b16 v[46:47], v41 offset:9216\n"
    "v_mfma_f32_16x16x32_bf16 v[76:79], v[48:51], v[180:183], v[76:79]\n"
    "s_add_u32 s56, s56, 1\n"
    "s_cmp_eq_u32 s56, 10\n"
    "s_cselect_b32 s56, 0, s56\n"
    "s_add_u32 m0, s40, 65536\n"
    "s_lshl_b32 s57, s56, 7\n"
    "buffer_load_dwordx4 v28, %[rx], s57 offen  lds\n"
    "s_waitcnt lgkmcnt(8)\n"
    "v_mfma_f32_16x16x32_bf16 v[80:83], v[52:55], v[176:179], v[80:83]\n"
    "s_setprio 0\n"
    "ds_read_b64_tr_b16 v[48:49], v42 offset:8192\n"
    "ds_read_b64_tr_b16 v[50:51], v42 offset:9216\n"
    "v_mfma_f32_16x16x32_bf16 v[84:87], v[52:55], v[180:183], v[84:87]\n"
    "s_waitcnt lgkmcnt(8)\n"
    "v_mfma_f32_16x16x32_bf16 v[88:91], v[56:59], v[176:179], v[88:91]\n"
    "ds_read_b64_tr_b16 v[52:53], v43 offset:8192\n"
    "ds_read_b64_tr_b16 v[54:55], v43 offset:9216\n"
    "v_mfma_f32_16x16x32_bf16 v[92:95], v[56:59], v[180:183], v[92:95]\n"
    "s_add_u32 m0, s40, 66560\n"
    "s_nop 0\n"
    "buffer_load_dwordx4 v29, %[rx], s57 offen  lds\n"
    "s_waitcnt lgkmcnt(6)\n"
    "v_mfma_f32_16x16x32_bf16 v[32:35], v[36:39], v[60:63], v[32:35]\n"
    "v_mfma_f32_16x16x32_bf16 v[64:67], v[60:63], v[184:187], v[64:67]\n"
    "s_waitcnt vmcnt(8)\n"
    "s_barrier\n"
    "s_setprio 3\n"
    "v_add_u32 v24, 0xffff0000, v24\n"
    "v_add_u32 v25, 0xffff0000, v25\n"
    "ds_read_b128 v[192:195], v24 offset:0\n"
    "ds_read_b128 v[196:199], v24 offset:2048\n"
    "v_add_u32 v40, 0xfffe0000, v40\n"
    "v_add_u32 v41, 0xfffe0000, v41\n"
    "v_add_u32 v42, 0xfffe0000, v42\n"
    "v_add_u32 v43, 0xfffe0000, v43\n"
    "ds_read_b64_tr_b16 v[56:57], v40 offset:16384\n"
    "ds_read_b64_tr_b16 v[58:59], v40 offset:17408\n"
    "v_mfma_f32_16x16x32_bf16 v[68:71], v[60:63], v[188:191], v[68:71]\n"
    "s_waitcnt lgkmcnt(8)\n"
    "v_mfma_f32_16x16x32_bf16 v[72:75], v[44:47], v[184:187], v[72:75]\n"
    "ds_read_b64_tr_b16 v[60:61], v41 offset:16384\n"
    "ds_read_b64_tr_b16 v[62:63], v41 offset:17408\n"
    "v_mfma_f32_16x16x32_bf16 v[76:79], v[44:47], v[188:191], v[76:79]\n"
    "s_add_u32 m0, s40, 81920\n"
    "s_mul_i32 s57, s56, 245760\n"
    "buffer_load_dwordx4 v30, %[rq], s57 offen  lds\n"
    "s_waitcnt lgkmcnt(8)\n"
    "v_mfma_f32_16x16x32_bf16 v[80:83], v[48:51], v[184:187], v[80:83]\n"
    "s_setprio 2\n"
    "ds_read_b64_tr_b16 v[44:45], v42 offset:16384\n"
    "ds_read_b64_tr_b16 v[46:47], v42 offset:17408\n"
    "v_mfma_f32_16x16x32_bf16 v[84:87], v[48:51], v[188:191], v[84:87]\n"
    "s_waitcnt lgkmcnt(8)\n"
    "v_mfma_f32_16x16x32_bf16 v[88:91], v[52:55], v[184:187], v[88:91]\n"
    "ds_read_b64_tr_b16 v[48:49], v43 offset:16384\n"
    "ds_read_b64_tr_b16 v[50:51], v43 offset:17408\n"
    "v_mfma_f32_16x16x32_bf16 v[92:95], v[52:55], v[188:191], v[92:95]\n"
    "s_add_u32 m0, s40, 82944\n"
    "s_add_u32 s57, s57, 15360\n"
    "buffer_load_dwordx4 v30, %[rq], s57 offen  lds\n"
    "s_waitcnt lgkmcnt(6)\n"
    "v_mfma_f32_16x16x32_bf16 v[32:35], v[36:39], v[56:59], v[32:35]\n"
    "v_mfma_f32_16x16x32_bf16 v[64:67], v[56:59], v[192:195], v[64:67]\n"
    "s_setprio 1\n"
    "ds_read_b128 v[200:203], v25 offset:0\n"
    "ds_read_b128 v[204:207], v25 offset:2048\n"
    "ds_read_b64_tr_b16 v[52:53], v40 offset:24576\n"
    "ds_read_b64_tr_b16 v[54:55], v40 offset:25600\n"
    "v_mfma_f32_16x16x32_bf16 v[68:71], v[56:59], v[196:199], v[68:71]\n"
    "s_waitcnt lgkmcnt(8)\n"
    "v_mfma_f32_16x16x32_bf16 v[72:75], v[60:63], v[192:195], v[72:75]\n"
    "ds_read_b64_tr_b16 v[56:57], v41 offset:24576\n"
    "ds_read_b64_tr_b16 v[58:59], v41 offset:25600\n"
    "v_mfma_f32_16x16x32_bf16 v[76:79], v[60:63], v[196:199], v[76:79]\n"
    "s_add_u32 s56, s56, 1\n"
    "s_cmp_eq_u32 s56, 10\n"
    "s_cselect_b32 s56, 0, s56\n"
    "s_add_u32 m0, s40, 98304\n"
    "s_lshl_b32 s57, s56, 7\n"
    "buffer_load_dwordx4 v28, %[rx], s57 offen  lds\n"
    "s_waitcnt lgkmcnt(8)\n"
    "v_mfma_f32_16x16x32_bf16 v[80:83], v[44:47], v[192:195], v[80:83]\n"
    "s_setprio 0\n"
    "ds_read_b64_tr_b16 v[60:61], v42 offset:24576\n"
    "ds_read_b64_tr_b16 v[62:63], v42 offset:25600\n"
    "v_mfma_f32_16x16x32_bf16 v[84:87], v[44:47], v[196:199], v[84:87]\n"
    "s_waitcnt lgkmcnt(8)\n"
    "v_mfma_f32_16x16x32_bf16 v[88:91], v[48:51], v[192:195], v[88:91]\n"
    "ds_read_b64_tr_b16 v[44:45], v43 offset:24576\n"
    "ds_read_b64_tr_b16 v[46:47], v43 offset:25600\n"
    "v_mfma_f32_16x16x32_bf16 v[92:95], v[48:51], v[196:199], v[92:95]\n"
    "s_add_u32 m0, s40, 99328\n"
    "s_nop 0\n"
    "buffer_load_dwordx4 v29, %[rx], s57 offen  lds\n"
    "s_waitcnt lgkmcnt(6)\n"
    "v_mfma_f32_16x16x32_bf16 v[32:35], v[36:39], v[52:55], v[32:35]\n"
    "v_mfma_f32_16x16x32_bf16 v[64:67], v[52:55], v[200:203], v[64:67]\n"
    "s_waitcnt vmcnt(6)\n"
    "s_barrier\n"
    "s_setprio 3\n"
    "ds_read_b128 v[208:211], v24 offset:32768\n"
    "ds_read_b128 v[212:215], v24 offset:34816\n"
    "ds_read_b64_tr_b16 v[48:49], v40 offset:49152\n"
    "ds_read_b64_tr_b16 v[50:51], v40 offset:50176\n"
    "v_mfma_f32_16x16x32_bf16 v[68:71], v[52:55], v[204:207], v[68:71]\n"
    "s_waitcnt lgkmcnt(8)\n"
    "v_mfma_f32_16x16x32_bf16 v[72:75], v[56:59], v[200:203], v[72:75]\n"
    "ds_read_b64_tr_b16 v[52:53], v41 offset:49152\n"
    "ds_read_b64_tr_b16 v[54:55], v41 offset:50176\n"
    "v_mfma_f32_16x16x32_bf16 v[76:79], v[56:59], v[204:207], v[76:79]\n"
    "s_add_u32 m0, s40, 114688\n"
    "s_mul_i32 s57, s56, 245760\n"
    "buffer_load_dwordx4 v30, %[rq], s57 offen  lds\n"
    "s_waitcnt lgkmcnt(8)\n"
    "v_mfma_f32_16x16x32_bf16 v[80:83], v[60:63], v[200:203], v[80:83]\n"
    "s_setprio 2\n"
    "ds_read_b64_tr_b16 v[56:57], v42 offset:49152\n"
    "ds_read_b64_tr_b16 v[58:59], v42 offset:50176\n"
    "v_mfma_f32_16x16x32_bf16 v[84:87], v[60:63], v[204:207], v[84:87]\n"
    "s_waitcnt lgkmcnt(8)\n"
    "v_mfma_f32_16x16x32_bf16 v[88:91], v[44:47], v[200:203], v[88:91]\n"
    "ds_read_b64_tr_b16 v[60:61], v43 offset:49152\n"
    "ds_read_b64_tr_b16 v[62:63], v43 offset:50176\n"
    "v_mfma_f32_16x16x32_bf16 v[92:95], v[44:47], v[204:207], v[92:95]\n"
    "s_add_u32 m0, s40, 115712\n"
    "s_add_u32 s57, s57, 15360\n"
    "buffer_load_dwordx4 v30, %[rq], s57 offen  lds\n"
    "s_waitcnt lgkmcnt(6)\n"
    "v_mfma_f32_16x16x32_bf16 v[32:35], v[36:39], v[48:51], v[32:35]\n"
    "v_mfma_f32_16x16x32_bf16 v[64:67], v[48:51], v[208:211], v[64:67]\n"
    "s_setprio 1\n"
    "ds_read_b128 v[216:219], v25 offset:32768\n"
    "ds_read_b128 v[220:223], v25 offset:34816\n"
    "ds_read_b64_tr_b16 v[44:45], v40 offset:57344\n"
    "ds_read_b64_tr_b16 v[46:47], v40 offset:58368\n"
    "v_mfma_f32_16x16x32_bf16 v[68:71], v[48:51], v[212:215], v[68:71]\n"
    "buffer_load_dwordx4 v[28:31], v26, %[rw], s52 offen sc1\n"
    "s_waitcnt lgkmcnt(8)\n"
    "v_mfma_f32_16x16x32_bf16 v[72:75], v[52:55], v[208:211], v[72:75]\n"
    "ds_read_b64_tr_b16 v[48:49], v41 offset:57344\n"
    "ds_read_b64_tr_b16 v[50:51], v41 offset:58368\n"
    "v_mfma_f32_16x16x32_bf16 v[76:79], v[52:55], v[212:215], v[76:79]\n"
    "s_add_u32 s56, s56, 1\n"
    "s_cmp_eq_u32 s56, 10\n"
    "s_cselect_b32 s56, 0, s56\n"
    "s_add_u32 m0, s40, 131072\n"
    "s_mul_i32 s57, s56, s55\n"
    "buffer_load_dwordx4 v23, %[r1], s57 offen  lds\n"
    "s_waitcnt lgkmcnt(8)\n"
    "v_mfma_f32_16x16x32_bf16 v[80:83], v[56:59], v[208:211], v[80:83]\n"
    "s_setprio 0\n"
    "ds_read_b64_tr_b16 v[52:53], v42 offset:57344\n"
    "ds_read_b64_tr_b16 v[54:55], v42 offset:58368\n"
    "v_mfma_f32_16x16x32_bf16 v[84:87], v[56:59], v[212:215], v[84:87]\n"
    "s_waitcnt lgkmcnt(8)\n"
    "v_mfma_f32_16x16x32_bf16 v[88:91], v[60:63], v[208:211], v[88:91]\n"
    "ds_read_b64_tr_b16 v[56:57], v43 offset:57344\n"
    "ds_read_b64_tr_b16 v[58:59], v43 offset:58368\n"
    "v_mfma_f32_16x16x32_bf16 v[92:95], v[60:63], v[212:215], v[92:95]\n"
    "s_add_u32 m0, s40, 132096\n"
    "s_add_u32 s57, s57, s59\n"
    "buffer_load_dwordx4 v23, %[r1], s57 offen  lds\n"
    "s_waitcnt lgkmcnt(6)\n"
    "v_mfma_f32_16x16x32_bf16 v[32:35], v[36:39], v[44:47], v[32:35]\n"
    "v_mfma_f32_16x16x32_bf16 v[64:67], v[44:47], v[216:219], v[64:67]\n"
    "s_waitcnt vmcnt(7)\n"
    "s_barrier\n"
    "s_setprio 3\n"
    "v_add_u32 v24, 0x10000, v24\n"
    "v_add_u32 v25, 0x10000, v25\n"
    "ds_read_b128 v[224:227], v24 offset:0\n"
    "ds_read_b128 v[228:231], v24 offset:2048\n"
    "v_add_u32 v40, 0x10000, v40\n"
    "v_add_u32 v41, 0x10000, v41\n"
    "v_add_u32 v42, 0x10000, v42\n"
    "v_add_u32 v43, 0x10000, v43\n"
    "ds_read_b64_tr_b16 v[60:61], v40 offset:16384\n"
    "ds_read_b64_tr_b16 v[62:63], v40 offset:17408\n"
    "v_mfma_f32_16x16x32_bf16 v[68:71], v[44:47], v[220:223], v[68:71]\n"
    "s_waitcnt lgkmcnt(8)\n"
    "v_mfma_f32_16x16x32_bf16 v[72:75], v[48:51], v[216:219], v[72:75]\n"
    "ds_read_b64_tr_b16 v[44:45], v41 offset:16384\n"
    "ds_read_b64_tr_b16 v[46:47], v41 offset:17408\n"
    "v_mfma_f32_16x16x32_bf16 v[76:79], v[48:51], v[220:223], v[76:79]\n"
    "s_add_u32 s56, s56, 1\n"
    "s_cmp_eq_u32 s56, 10\n"
    "s_cselect_b32 s56, 0, s56\n"
    "s_add_u32 m0, s40, 0\n"
    "s_mul_i32 s57, s56, s55\n"
    "buffer_load_dwordx4 v23, %[r1], s57 offen  lds\n"
    "s_waitcnt lgkmcnt(8)\n"
    "v_mfma_f32_16x16x32_bf16 v[80:83], v[52:55], v[216:219], v[80:83]\n"
    "s_setprio 2\n"
    "ds_read_b64_tr_b16 v[48:49], v42 offset:16384\n"
    "ds_read_b64_tr_b16 v[50:51], v42 offset:17408\n"
    "v_mfma_f32_16x16x32_bf16 v[84:87], v[52:55], v[220:223], v[84:87]\n"
    "s_waitcnt lgkmcnt(8)\n"
    "v_mfma_f32_16x16x32_bf16 v[88:91], v[56:59], v[216:219], v[88:91]\n"
    "ds_read_b64_tr_b16 v[52:53], v43 offset:16384\n"
    "ds_read_b64_tr_b16 v[54:55], v43 offset:17408\n"
    "v_mfma_f32_16x16x32_bf16 v[92:95], v[56:59], v[220:223], v[92:95]\n"
    "s_add_u32 m0, s40, 1024\n"
    "s_add_u32 s57, s57, s59\n"
    "buffer_load_dwordx4 v23, %[r1], s57 offen  lds\n"
    "s_waitcnt lgkmcnt(6)\n"
    "v_mfma_f32_16x16x32_bf16 v[32:35], v[36:39], v[60:63], v[32:35]\n"
    "v_mfma_f32_16x16x32_bf16 v[64:67], v[60:63], v[224:227], v[64:67]\n"
    "s_setprio 1\n"
    "ds_read_b128 v[232:235], v25 offset:0\n"
    "ds_read_b128 v[236:239], v25 offset:2048\n"
    "ds_read_b64_tr_b16 v[56:57], v40 offset:24576\n"
    "ds_read_b64_tr_b16 v[58:59], v40 offset:25600\n"
    "v_mfma_f32_16x16x32_bf16 v[68:71], v[60:63], v[228:231], v[68:71]\n"
    "s_waitcnt lgkmcnt(8)\n"
    "v_mfma_f32_16x16x32_bf16 v[72:75], v[44:47], v[224:227], v[72:75]\n"
    "ds_read_b64_tr_b16 v[60:61], v41 offset:24576\n"
    "ds_read_b64_tr_b16 v[62:63], v41 offset:25600\n"
    "v_mfma_f32_16x16x32_bf16 v[76:79], v[44:47], v[228:231], v[76:79]\n"
    "s_add_u32 s56, s56, 1\n"
    "s_cmp_eq_u32 s56, 10\n"
    "s_cselect_b32 s56, 0, s56\n"
    "s_add_u32 m0, s40, 16384\n"
    "s_mul_i32 s57, s56, s55\n"
    "buffer_load_dwordx4 v23, %[r1], s57 offen  lds\n"
    "s_waitcnt lgkmcnt(8)\n"
    "v_mfma_f32_16x16x32_bf16 v[80:83], v[48:51], v[224:227], v[80:83]\n"
    "s_setprio 0\n"
    "ds_read_b64_tr_b16 v[44:45], v42 offset:24576\n"
    "ds_read_b64_tr_b16 v[46:47], v42 offset:25600\n"
    "v_mfma_f32_16x16x32_bf16 v[84:87], v[48:51], v[228:231], v[84:87]\n"
    "s_waitcnt lgkmcnt(8)\n"
    "v_mfma_f32_16x16x32_bf16 v[88:91], v[52:55], v[224:227], v[88:91]\n"
    "ds_read_b64_tr_b16 v[48:49], v43 offset:24576\n"
    "ds_read_b64_tr_b16 v[50:51], v43 offset:25600\n"
    "v_mfma_f32_16x16x32_bf16 v[92:95], v[52:55], v[228:231], v[92:95]\n"
    "s_add_u32 m0, s40, 17408\n"
    "s_add_u32 s57, s57, s59\n"
    "buffer_load_dwordx4 v23, %[r1], s57 offen  lds\n"
    "s_waitcnt lgkmcnt(6)\n"
    "v_mfma_f32_16x16x32_bf16 v[32:35], v[36:39], v[56:59], v[32:35]\n"
    "v_mfma_f32_16x16x32_bf16 v[64:67], v[56:59], v[232:235], v[64:67]\n"
    "s_waitcnt vmcnt(7)\n"
    "s_barrier\n"
    "s_setprio 3\n"
    "ds_read_b128 v[240:243], v24 offset:32768\n"
    "ds_read_b128 v[244:247], v24 offset:34816\n"
    "ds_read_b64_tr_b16 v[52:53], v40 offset:49152\n"
    "ds_read_b64_tr_b16 v[54:55], v40 offset:50176\n"
    "v_mfma_f32_16x16x32_bf16 v[68:71], v[56:59], v[236:239], v[68:71]\n"
    "s_waitcnt lgkmcnt(8)\n"
    "v_mfma_f32_16x16x32_bf16 v[72:75], v[60:63], v[232:235], v[72:75]\n"
    "ds_read_b128 v[248:251], v25 offset:32768\n"
    "ds_read_b128 v[252:255], v25 offset:34816\n"
    "ds_read_b64_tr_b16 v[56:57], v40 offset:57344\n"
    "ds_read_b64_tr_b16 v[58:59], v40 offset:58368\n"
    "v_mfma_f32_16x16x32_bf16 v[76:79], v[60:63], v[236:239], v[76:79]\n"
    "s_add_u32 s56, s56, 1\n"
    "s_cmp_eq_u32 s56, 10\n"
    "s_cselect_b32 s56, 0, s56\n"
    "s_add_u32 m0, s40, 32768\n"
    "s_mul_i32 s57, s56, s55\n"
    "buffer_load_dwordx4 v23, %[r1], s57 offen  lds\n"
    "s_waitcnt lgkmcnt(10)\n"
    "v_mfma_f32_16x16x32_bf16 v[80:83], v[44:47], v[232:235], v[80:83]\n"
    "s_setprio 2\n"
    "ds_read_b64_tr_b16 v[60:61], v41 offset:49152\n"
    "ds_read_b64_tr_b16 v[62:63], v41 offset:50176\n"
    "v_mfma_f32_16x16x32_bf16 v[84:87], v[44:47], v[236:239], v[84:87]\n"
    "s_waitcnt vmcnt(7)\n"
    "s_mov_b32 s58, 1048576\n"
    "3:\n"
    "v_cmp_ne_u32 s[62:63], s60, v29\n"
    "s_mov_b64 vcc, s[62:63]\n"
    "v_cmp_ne_u32 s[62:63], s61, v31\n"
    "s_or_b64 vcc, vcc, s[62:63]\n"
    "s_cbranch_scc0 4f\n"
    "s_sub_u32 s58, s58, 1\n"
    "s_cbranch_scc1 4f\n"
    "s_sleep 2\n"
    "buffer_load_dwordx4 v[28:31], v26, %[rw], s52 offen sc1\n"
    "s_waitcnt vmcnt(0)\n"
    "s_branch 3b\n"
    "4:\n"
    "s_waitcnt lgkmcnt(10)\n"
    "v_mfma_f32_16x16x32_bf16 v[88:91], v[48:51], v[232:235], v[88:91]\n"
    "ds_read_b64_tr_b16 v[44:45], v42 offset:49152\n"
    "ds_read_b64_tr_b16 v[46:47], v42 offset:50176\n"
    "v_add_f32 v27, 0x3727c5ac, v30\n"
    "v_rsq_f32 v27, v27\n"
    "v_mfma_f32_16x16x32_bf16 v[92:95], v[48:51], v[236:239], v[92:95]\n"
    "v_mov_b32 v24, v28\n"
    "v_mul_f32 v29, v30, v27\n"
    "s_add_u32 m0, s40, 33792\n"
    "s_add_u32 s57, s57, s59\n"
    "buffer_load_dwordx4 v23, %[r1], s57 offen  lds\n"
    "s_waitcnt lgkmcnt(8)\n"
    "v_mfma_f32_16x16x32_bf16 v[32:35], v[36:39], v[52:55], v[32:35]\n"
    "v_mfma_f32_16x16x32_bf16 v[64:67], v[52:55], v[240:243], v[64:67]\n"
    "s_setprio 1\n"
    "ds_read_b64_tr_b16 v[48:49], v43 offset:49152\n"
    "ds_read_b64_tr_b16 v[50:51], v43 offset:50176\n"
    "v_fmaak_f32 v29, v29, v27, 0x3727c5ac\n"
    "v_rsq_f32 v29, v29\n"
    "v_mfma_f32_16x16x32_bf16 v[68:71], v[52:55], v[244:247], v[68:71]\n"
    "v_mul_f32 v30, s49, v27\n"
    "v_mul_f32 v31, v29, v27\n"
    "v_rcp_f32 v29, v31\n"
    "s_waitcnt lgkmcnt(6)\n"
    "v_mfma_f32_16x16x32_bf16 v[32:35], v[36:39], v[56:59], v[32:35]\n"
    "v_mfma_f32_16x16x32_bf16 v[64:67], v[56:59], v[248:251], v[64:67]\n"
    "ds_read_b64_tr_b16 v[52:53], v41 offset:57344\n"
    "ds_read_b64_tr_b16 v[54:55], v41 offset:58368\n"
    "v_mov_b32 v26, v30\n"
    "v_mov_b32 v8, v27\n"
    "v_mfma_f32_16x16x32_bf16 v[68:71], v[56:59], v[252:255], v[68:71]\n"
    "v_cvt_pk_bf16_f32 v27, v29, v29\n"
    "v_lshlrev_b32 v27, 16, v27\n"
    "s_add_u32 s56, s56, 1\n"
    "s_cmp_eq_u32 s56, 10\n"
    "s_cselect_b32 s56, 0, s56\n"
    "s_add_u32 m0, s40, 49152\n"
    "s_mul_i32 s57, s56, s55\n"
    "buffer_load_dwordx4 v23, %[r1], s57 offen  lds\n"
    "s_waitcnt lgkmcnt(6)\n"
    "v_mfma_f32_16x16x32_bf16 v[72:75], v[60:63], v[240:243], v[72:75]\n"
    "s_setprio 0\n"
    "ds_read_b64_tr_b16 v[56:57], v42 offset:57344\n"
    "ds_read_b64_tr_b16 v[58:59], v42 offset:58368\n"
    "v_sub_f32 v27, v29, v27\n"
    "v_cvt_pk_bf16_f32 v29, v29, v27\n"
    "v_mfma_f32_16x16x32_bf16 v[76:79], v[60:63], v[244:247], v[76:79]\n"
    "v_mov_b32 v27, v31\n"
    "v_mov_b32 v25, v29\n"
    "s_nop 1\n"
    "v_cvt_pk_bf16_f32 v33, v32, v32\n"
    "v_lshlrev_b32 v33, 16, v33\n"
    "v_sub_f32 v33, v32, v33\n"
    "v_cvt_pk_bf16_f32 v32, v32, v33\n"
    "ds_write_b32 v21, v32\n"
    "s_waitcnt lgkmcnt(7)\n"
    "v_mfma_f32_16x16x32_bf16 v[80:83], v[44:47], v[240:243], v[80:83]\n"
    "ds_read_b64_tr_b16 v[60:61], v43 offset:57344\n"
    "ds_read_b64_tr_b16 v[62:63], v43 offset:58368\n"
    "v_permlane16_swap_b32 v28, v24\n"
    "v_permlane16_swap_b32 v30, v26\n"
    "v_mfma_f32_16x16x32_bf16 v[84:87], v[44:47], v[244:247], v[84:87]\n"
    "v_permlane16_swap_b32 v31, v27\n"
    "v_permlane16_swap_b32 v29, v25\n"
    "s_add_u32 m0, s40, 50176\n"
    "s_add_u32 s57, s57, s59\n"
    "buffer_load_dwordx4 v23, %[r1], s57 offen  lds\n"
    "s_waitcnt lgkmcnt(7)\n"
    "v_mfma_f32_16x16x32_bf16 v[88:91], v[48:51], v[240:243], v[88:91]\n"
    "v_perm_b32 v28, 0, v28, v22\n"
    "v_perm_b32 v24, 0, v24, v22\n"
    "v_mfma_f32_16x16x32_bf16 v[92:95], v[48:51], v[244:247], v[92:95]\n"
    "v_perm_b32 v29, 0, v29, v22\n"
    "v_perm_b32 v25, 0, v25, v22\n"
    "s_waitcnt vmcnt(8) lgkmcnt(0)\n"
    "s_barrier\n"
    "v_xor_b32 v32, 0, v21\n"
    "v_xor_b32 v33, 64, v21\n"
    "v_xor_b32 v34, 128, v21\n"
    "v_xor_b32 v35, 192, v21\n"
    "ds_read_b32 v44, v32\n"
    "ds_read_b32 v46, v33\n"
    "ds_read_b32 v48, v34\n"
    "ds_read_b32 v50, v35\n"
    "s_add_u32 s56, s56, 1\n"
    "s_cmp_eq_u32 s56, 10\n"
    "s_cselect_b32 s56, 0, s56\n"
    "s_add_u32 m0, s40, 65536\n"
    "s_mul_i32 s57, s56, s55\n"
    "buffer_load_dwordx4 v23, %[r1], s57 offen  lds\n"
    "v_mfma_f32_16x16x32_bf16 v[72:75], v[52:55], v[248:251], v[72:75]\n"
    "v_mov_b32 v45, 0\n"
    "v_mov_b32 v47, 0\n"
    "v_mfma_f32_16x16x32_bf16 v[76:79], v[52:55], v[252:255], v[76:79]\n"
    "v_mov_b32 v49, 0\n"
    "v_mov_b32 v51, 0\n"
    "s_add_u32 m0, s40, 66560\n"
    "s_add_u32 s57, s57, s59\n"
    "buffer_load_dwordx4 v23, %[r1], s57 offen  lds\n"
    "v_mfma_f32_16x16x32_bf16 v[80:83], v[56:59], v[248:251], v[80:83]\n"
    "v_add_u32 v40, 0x10000, v40\n"
    "v_add_u32 v41, 0x10000, v41\n"
    "v_add_u32 v42, 0x10000, v42\n"
    "v_add_u32 v43, 0x10000, v43\n"
    "ds_read_b64_tr_b16 v[52:53], v40 offset:0\n"
    "ds_read_b64_tr_b16 v[54:55], v40 offset:1024\n"
    "v_mfma_f32_16x16x32_bf16 v[84:87], v[56:59], v[252:255], v[84:87]\n"
    "s_add_u32 s56, s56, 1\n"
    "s_cmp_eq_u32 s56, 10\n"
    "s_cselect_b32 s56, 0, s56\n"
    "s_add_u32 m0, s40, 81920\n"
    "s_mul_i32 s57, s56, s55\n"
    "buffer_load_dwordx4 v23, %[r1], s57 offen  lds\n"
    "v_mfma_f32_16x16x32_bf16 v[88:91], v[60:63], v[248:251], v[88:91]\n"
    "ds_read_b64_tr_b16 v[56:57], v41 offset:0\n"
    "ds_read_b64_tr_b16 v[58:59], v41 offset:1024\n"
    "v_mfma_f32_16x16x32_bf16 v[92:95], v[60:63], v[252:255], v[92:95]\n"
    "s_add_u32 m0, s40, 82944\n"
    "s_add_u32 s57, s57, s59\n"
    "buffer_load_dwordx4 v23, %[r1], s57 offen  lds\n"
    "s_waitcnt lgkmcnt(4)\n"
    "v_mfma_f32_16x16x16_bf16 v[64:67], v[44:45], v[28:29], v[64:67]\n"
    "ds_read_b64_tr_b16 v[60:61], v42 offset:0\n"
    "ds_read_b64_tr_b16 v[62:63], v42 offset:1024\n"
    "v_mfma_f32_16x16x16_bf16 v[72:75], v[46:47], v[28:29], v[72:75]\n"
    "v_mfma_f32_16x16x16_bf16 v[68:71], v[44:45], v[24:25], v[68:71]\n"
    "v_mfma_f32_16x16x16_bf16 v[76:79], v[46:47], v[24:25], v[76:79]\n"
    "v_mfma_f32_16x16x16_bf16 v[80:83], v[48:49], v[28:29], v[80:83]\n"
    "v_mfma_f32_16x16x16_bf16 v[88:91], v[50:51], v[28:29], v[88:91]\n"
    "v_mfma_f32_16x16x16_bf16 v[84:87], v[48:49], v[24:25], v[84:87]\n"
    "v_mfma_f32_16x16x16_bf16 v[92:95], v[50:51], v[24:25], v[92:95]\n"
    "ds_read_b64_tr_b16 v[44:45], v43 offset:0\n"
    "ds_read_b64_tr_b16 v[46:47], v43 offset:1024\n"
    "v_mul_f32 v64, v30, v64\n"
    "v_mul_f32 v65, v30, v65\n"
    "v_mul_f32 v66, v30, v66\n"
    "v_mul_f32 v67, v30, v67\n"
    "v_mul_f32 v72, v30, v72\n"
    "v_mul_f32 v73, v30, v73\n"
    "v_mul_f32 v74, v30, v74\n"
    "v_mul_f32 v75, v30, v75\n"
    "v_cvt_pk_bf16_f32 v64, v64, v65\n"
    "v_cvt_pk_bf16_f32 v65, v66, v67\n"
    "v_cvt_pk_bf16_f32 v66, v72, v73\n"
    "v_cvt_pk_bf16_f32 v67, v74, v75\n"
    "v_mul_f32 v68, v26, v68\n"
    "v_mul_f32 v69, v26, v69\n"
    "v_mul_f32 v70, v26, v70\n"
    "v_mul_f32 v71, v26, v71\n"
    "v_permlane16_swap_b32 v64, v66\n"
    "v_permlane16_swap_b32 v65, v67\n"
    "buffer_store_dwordx4 v[64:67], v0, %[rw], s50 offen sc1\n"
    "v_mul_f32 v76, v26, v76\n"
    "v_mul_f32 v77, v26, v77\n"
    "v_mul_f32 v78, v26, v78\n"
    "v_mul_f32 v79, v26, v79\n"
    "v_cvt_pk_bf16_f32 v68, v68, v69\n"
    "v_cvt_pk_bf16_f32 v69, v70, v71\n"
    "v_cvt_pk_bf16_f32 v70, v76, v77\n"
    "v_cvt_pk_bf16_f32 v71, v78, v79\n"
    "v_mul_f32 v80, v30, v80\n"
    "v_mul_f32 v81, v30, v81\n"
    "v_mul_f32 v82, v30, v82\n"
    "v_mul_f32 v83, v30, v83\n"
    "v_permlane16_swap_b32 v68, v70\n"
    "v_permlane16_swap_b32 v69, v71\n"
    "buffer_store_dwordx4 v[68:71], v1, %[rw], s50 offen sc1\n"
    "v_mul_f32 v88, v30, v88\n"
    "v_mul_f32 v89, v30, v89\n"
    "v_mul_f32 v90, v30, v90\n"
    "v_mul_f32 v91, v30, v91\n"
    "v_cvt_pk_bf16_f32 v16, v80, v81\n"
    "v_cvt_pk_bf16_f32 v17, v82, v83\n"
    "v_cvt_pk_bf16_f32 v18, v88, v89\n"
    "v_cvt_pk_bf16_f32 v19, v90, v91\n"
    "v_mul_f32 v84, v26, v84\n"
    "v_mul_f32 v85, v26, v85\n"
    "v_mul_f32 v86, v26, v86\n"
    "v_mul_f32 v87, v26, v87\n"
    "v_permlane16_swap_b32 v16, v18\n"
    "v_permlane16_swap_b32 v17, v19\n"
    "v_mul_f32 v92, v26, v92\n"
    "v_mul_f32 v93, v26, v93\n"
    "v_mul_f32 v94, v26, v94\n"
    "v_mul_f32 v95, v26, v95\n"
    "v_cvt_pk_bf16_f32 v8, v84, v85\n"
    "v_cvt_pk_bf16_f32 v9, v86, v87\n"
    "v_cvt_pk_bf16_f32 v10, v92, v93\n"
    "v_cvt_pk_bf16_f32 v11, v94, v95\n"
    "s_nop 1\n"
    "v_permlane16_swap_b32 v8, v10\n"
    "v_permlane16_swap_b32 v9, v11\n"
    "s_branch .Lp1_h\n"
    ".Lp1_v:\n"
    "s_barrier\n"
    "ds_read_b128 v[96:99], v24 offset:32768\n"
    "ds_read_b128 v[100:103], v24 offset:34816\n"
    "ds_read_b64_tr_b16 v[44:45], v40 offset:49152\n"
    "ds_read_b64_tr_b16 v[46:47], v40 offset:50176\n"
    "ds_read_b64_tr_b16 v[48:49], v41 offset:49152\n"
    "ds_read_b64_tr_b16 v[50:51], v41 offset:50176\n"
    "ds_read_b64_tr_b16 v[52:53], v42 offset:49152\n"
    "ds_read_b64_tr_b16 v[54:55], v42 offset:50176\n"
    "ds_read_b64_tr_b16 v[56:57], v43 offset:49152\n"
    "ds_read_b64_tr_b16 v[58:59], v43 offset:50176\n"
    "s_waitcnt lgkmcnt(6)\n"
    "v_mfma_f32_16x16x32_bf16 v[32:35], v[36:39], v[44:47], 0\n"
    "v_mfma_f32_16x16x32_bf16 v[64:67], v[96:99], v[44:47], 0\n"
    "s_setprio 1\n"
    "ds_read_b128 v[104:107], v25 offset:32768\n"
    "ds_read_b128 v[108:111], v25 offset:34816\n"
    "ds_read_b64_tr_b16 v[60:61], v40 offset:57344\n"
    "ds_read_b64_tr_b16 v[62:63], v40 offset:58368\n"
    "s_and_b32 s64, s43, 15\n"
    "s_lshr_b32 s65, s40, 11\n"
    "v_mfma_f32_16x16x32_bf16 v[68:71], v[100:103], v[44:47], 0\n"
    "v_lshrrev_b32 v255, 5, v6\n"
    "s_lshl3_add_u32 s64, s64, s65\n"
    "s_add_u32 s56, s56, 1\n"
    "s_cmp_eq_u32 s56, 10\n"
    "s_cselect_b32 s56, 0, s56\n"
    "s_add_u32 m0, s40, 16384\n"
    "s_lshl_b32 s57, s56, 7\n"
    "buffer_load_dwordx4 v28, %[rx], s57 offen  lds\n"
    "s_waitcnt lgkmcnt(8)\n"
    "v_mfma_f32_16x16x32_bf16 v[72:75], v[96:99], v[48:51], 0\n"
    "ds_read_b64_tr_b16 v[44:45], v41 offset:57344\n"
    "ds_read_b64_tr_b16 v[46:47], v41 offset:58368\n"
    "v_mul_u32_u24 v255, 3456, v255\n"
    "s_mul_i32 s64, s64, 17280\n"
    "v_mfma_f32_16x16x32_bf16 v[76:79], v[100:103], v[48:51], 0\n"
    "v_and_b32 v252, 16, v6\n"
    "s_lshl_b32 s65, s44, 8\n"
    "s_add_u32 m0, s40, 17408\n"
    "s_nop 0\n"
    "buffer_load_dwordx4 v29, %[rx], s57 offen  lds\n"
    "s_waitcnt lgkmcnt(8)\n"
    "v_mfma_f32_16x16x32_bf16 v[80:83], v[96:99], v[52:55], 0\n"
    "s_setprio 0\n"
    "ds_read_b64_tr_b16 v[48:49], v42 offset:57344\n"
    "ds_read_b64_tr_b16 v[50:51], v42 offset:58368\n"
    "v_lshl_add_u32 v255, v252, 3, v255\n"
    "s_add_u32 s64, s64, s65\n"
    "v_mfma_f32_16x16x32_bf16 v[84:87], v[100:103], v[52:55], 0\n"
    "v_add_u32 v255, s64, v255\n"
    "s_mov_b64 exec, 1023\nbuffer_load_dword v255, v255, %[r1], 0 offen\ns_mov_b64 exec, -1\n"
    "s_add_u32 m0, s40, 32768\n"
    "s_mul_i32 s57, s56, 245760\n"
    "buffer_load_dwordx4 v30, %[rq], s57 offen  lds\n"
    "s_waitcnt lgkmcnt(8)\n"
    "v_mfma_f32_16x16x32_bf16 v[88:91], v[96:99], v[56:59], 0\n"
    "ds_read_b64_tr_b16 v[52:53], v43 offset:57344\n"
    "ds_read_b64_tr_b16 v[54:55], v43 offset:58368\n"
    "v_mfma_f32_16x16x32_bf16 v[92:95], v[100:103], v[56:59], 0\n"
    "s_add_u32 m0, s40, 33792\n"
    "s_add_u32 s57, s57, 15360\n"
    "buffer_load_dwordx4 v30, %[rq], s57 offen  lds\n"
    "s_waitcnt lgkmcnt(6)\n"
    "v_mfma_f32_16x16x32_bf16 v[32:35], v[36:39], v[60:63], v[32:35]\n"
    "v_mfma_f32_16x16x32_bf16 v[64:67], v[104:107], v[60:63], v[64:67]\n"
    "s_waitcnt vmcnt(5)\n"
    "s_barrier\n"
    "s_setprio 3\n"
    "v_add_u32 v24, 0x10000, v24\n"
    "v_add_u32 v25, 0x10000, v25\n"
    "ds_read_b128 v[112:115], v24 offset:0\n"
    "ds_read_b128 v[116:119], v24 offset:2048\n"
    "v_add_u32 v40, 0xffff0000, v40\n"
    "v_add_u32 v41, 0xffff0000, v41\n"
    "v_add_u32 v42, 0xffff0000, v42\n"
    "v_add_u32 v43, 0xffff0000, v43\n"
    "ds_read_b64_tr_b16 v[56:57], v40 offset:0\n"
    "ds_read_b64_tr_b16 v[58:59], v40 offset:1024\n"
    "v_mfma_f32_16x16x32_bf16 v[68:71], v[108:111], v[60:63], v[68:71]\n"
    "s_add_u32 s56, s56, 1\n"
    "s_cmp_eq_u32 s56, 10\n"
    "s_cselect_b32 s56, 0, s56\n"
    "s_add_u32 m0, s40, 49152\n"
    "s_lshl_b32 s57, s56, 7\n"
    "buffer_load_dwordx4 v28, %[rx], s57 offen  lds\n"
    "s_waitcnt lgkmcnt(8)\n"
    "v_mfma_f32_16x16x32_bf16 v[72:75], v[104:107], v[44:47], v[72:75]\n"
    "ds_read_b64_tr_b16 v[60:61], v41 offset:0\n"
    "ds_read_b64_tr_b16 v[62:63], v41 offset:1024\n"
    "v_readfirstlane_b32 s60, v8\n"
    "v_readfirstlane_b32 s61, v9\n"
    "v_mfma_f32_16x16x32_bf16 v[76:79], v[108:111], v[44:47], v[76:79]\n"
    "v_dot2c_f32_bf16 v10, v36, v0\n"
    "v_dot2c_f32_bf16 v11, v0, v0\n"
    "s_add_u32 m0, s40, 50176\n"
    "s_nop 0\n"
    "buffer_load_dwordx4 v29, %[rx], s57 offen  lds\n"
    "s_waitcnt lgkmcnt(8)\n"
    "v_mfma_f32_16x16x32_bf16 v[80:83], v[104:107], v[48:51], v[80:83]\n"
    "s_setprio 2\n"
    "ds_read_b64_tr_b16 v[44:45], v42 offset:0\n"
    "ds_read_b64_tr_b16 v[46:47], v42 offset:1024\n"
    "v_dot2c_f32_bf16 v10, v36, v1\n"
    "v_dot2c_f32_bf16 v11, v1, v1\n"
    "v_mfma_f32_16x16x32_bf16 v[84:87], v[108:111], v[48:51], v[84:87]\n"
    "v_dot2c_f32_bf16 v10, v36, v2\n"
    "v_dot2c_f32_bf16 v11, v2, v2\n"
    "s_waitcnt lgkmcnt(8)\n"
    "v_mfma_f32_16x16x32_bf16 v[88:91], v[104:107], v[52:55], v[88:91]\n"
    "ds_read_b64_tr_b16 v[48:49], v43 offset:0\n"
    "ds_read_b64_tr_b16 v[50:51], v43 offset:1024\n"
    "v_dot2c_f32_bf16 v10, v36, v3\n"
    "v_dot2c_f32_bf16 v11, v3, v3\n"
    "v_mfma_f32_16x16x32_bf16 v[92:95], v[108:111], v[52:55], v[92:95]\n"
    "v_dot2c_f32_bf16 v10, v36, v4\n"
    "v_dot2c_f32_bf16 v11, v4, v4\n"
    "s_add_u32 m0, s40, 65536\n"
    "s_mul_i32 s57, s56, 245760\n"
    "buffer_load_dwordx4 v30, %[rq], s57 offen  lds\n"
    "s_waitcnt lgkmcnt(6)\n"
    "v_mfma_f32_16x16x32_bf16 v[32:35], v[36:39], v[56:59], v[32:35]\n"
    "v_mfma_f32_16x16x32_bf16 v[64:67], v[112:115], v[56:59], v[64:67]\n"
    "s_setprio 1\n"
    "ds_read_b128 v[120:123], v25 offset:0\n"
    "ds_read_b128 v[124:127], v25 offset:2048\n"
    "ds_read_b64_tr_b16 v[52:53], v40 offset:8192\n"
    "ds_read_b64_tr_b16 v[54:55], v40 offset:9216\n"
    "s_mul_i32 s61, s61, 0x4c957f2d\n"
    "s_mul_i32 s58, s60, 0x5851f42d\n"
    "v_mfma_f32_16x16x32_bf16 v[68:71], v[116:119], v[56:59], v[68:71]\n"
    "s_add_u32 s61, s61, s58\n"
    "v_add_f32_dpp v10, v10, v10 row_shr:1 row_mask:0xf bank_mask:0xf bound_ctrl:1\n"
    "s_waitcnt lgkmcnt(8)\n"
    "v_mfma_f32_16x16x32_bf16 v[72:75], v[112:115], v[60:63], v[72:75]\n"
    "ds_read_b64_tr_b16 v[56:57], v41 offset:8192\n"
    "ds_read_b64_tr_b16 v[58:59], v41 offset:9216\n"
    "v_add_f32_dpp v11, v11, v11 row_shr:1 row_mask:0xf bank_mask:0xf bound_ctrl:1\n"
    "s_mul_hi_u32 s58, s60, 0x4c957f2d\n"
    "v_mfma_f32_16x16x32_bf16 v[76:79], v[116:119], v[60:63], v[76:79]\n"
    "v_add_f32_dpp v10, v10, v10 row_shr:2 row_mask:0xf bank_mask:0xf bound_ctrl:1\n"
    "v_add_f32_dpp v11, v11, v11 row_shr:2 row_mask:0xf bank_mask:0xf bound_ctrl:1\n"
    "s_add_u32 m0, s40, 66560\n"
    "s_add_u32 s57, s57, 15360\n"
    "buffer_load_dwordx4 v30, %[rq], s57 offen  lds\n"
    "s_waitcnt lgkmcnt(8)\n"
    "v_mfma_f32_16x16x32_bf16 v[80:83], v[112:115], v[44:47], v[80:83]\n"
    "s_setprio 0\n"
    "ds_read_b64_tr_b16 v[60:61], v42 offset:8192\n"
    "ds_read_b64_tr_b16 v[62:63], v42 offset:9216\n"
    "s_add_u32 s61, s61, s58\n"
    "v_add_f32_dpp v10, v10, v10 row_shr:4 row_mask:0xf bank_mask:0xf bound_ctrl:1\n"
    "v_mfma_f32_16x16x32_bf16 v[84:87], v[116:119], v[44:47], v[84:87]\n"
    "v_add_f32_dpp v11, v11, v11 row_shr:4 row_mask:0xf bank_mask:0xf bound_ctrl:1\n"
    "s_mul_i32 s60, s60, 0x4c957f2d\n"
    "s_waitcnt lgkmcnt(8)\n"
    "v_mfma_f32_16x16x32_bf16 v[88:91], v[112:115], v[48:51], v[88:91]\n"
    "ds_read_b64_tr_b16 v[44:45], v43 offset:8192\n"
    "ds_read_b64_tr_b16 v[46:47], v43 offset:9216\n"
    "v_add_f32_dpp v10, v10, v10 row_shr:8 row_mask:0xf bank_mask:0xf bound_ctrl:1\n"
    "v_add_f32_dpp v11, v11, v11 row_shr:8 row_mask:0xf bank_mask:0xf bound_ctrl:1\n"
    "v_mfma_f32_16x16x32_bf16 v[92:95], v[116:119], v[48:51], v[92:95]\n"
    "s_add_u32 s60, s60, 0xf767814f\ns_addc_u32 s61, s61, 0x14057b7e\n"
    "v_add_f32_dpp v10, v10, v10 row_bcast:15 row_mask:0xa bank_mask:0xf bound_ctrl:1\n"
    "s_add_u32 s56, s56, 1\n"
    "s_cmp_eq_u32 s56, 10\n"
    "s_cselect_b32 s56, 0, s56\n"
    "s_add_u32 m0, s40, 81920\n"
    "s_lshl_b32 s57, s56, 7\n"
    "buffer_load_dwordx4 v28, %[rx], s57 offen  lds\n"
    "s_waitcnt lgkmcnt(6)\n"
    "v_mfma_f32_16x16x32_bf16 v[32:35], v[36:39], v[52:55], v[32:35]\n"
    "v_mfma_f32_16x16x32_bf16 v[64:67], v[120:123], v[52:55], v[64:67]\n"
    "s_waitcnt vmcnt(5)\n"
    "s_barrier\n"
    "s_setprio 3\n"
    "v_add_u32 v24, 0xfffe0000, v24\n"
    "v_add_u32 v25, 0xfffe0000, v25\n"
    "ds_read_b128 v[128:131], v24 offset:16384\n"
    "ds_read_b128 v[132:135], v24 offset:18432\n"
    "ds_read_b64_tr_b16 v[48:49], v40 offset:32768\n"
    "ds_read_b64_tr_b16 v[50:51], v40 offset:33792\n"
    "v_add_f32_dpp v11, v11, v11 row_bcast:15 row_mask:0xa bank_mask:0xf bound_ctrl:1\n"
    "v_mov_b32 v13, s60\n"
    "v_mfma_f32_16x16x32_bf16 v[68:71], v[124:127], v[52:55], v[68:71]\n"
    "v_add_f32_dpp v10, v10, v10 row_bcast:31 row_mask:0xc bank_mask:0xf bound_ctrl:1\n"
    "v_add_f32_dpp v11, v11, v11 row_bcast:31 row_mask:0xc bank_mask:0xf bound_ctrl:1\n"
    "s_add_u32 m0, s40, 82944\n"
    "s_nop 0\n"
    "buffer_load_dwordx4 v29, %[rx], s57 offen  lds\n"
    "s_waitcnt lgkmcnt(8)\n"
    "v_mfma_f32_16x16x32_bf16 v[72:75], v[120:123], v[56:59], v[72:75]\n"
    "ds_read_b64_tr_b16 v[52:53], v41 offset:32768\n"
    "ds_read_b64_tr_b16 v[54:55], v41 offset:33792\n"
    "v_mov_b32 v15, s61\n"
    "v_mul_f32 v10, 0xbacccccd, v10\n"
    "v_mfma_f32_16x16x32_bf16 v[76:79], v[124:127], v[56:59], v[76:79]\n"
    "v_mul_f32 v11, 0x3acccccd, v11\n"
    "v_cvt_pk_bf16_f32 v12, v10, v10\n"
    "s_add_u32 m0, s40, 98304\n"
    "s_mul_i32 s57, s56, 245760\n"
    "buffer_load_dwordx4 v30, %[rq], s57 offen  lds\n"
    "s_waitcnt lgkmcnt(8)\n"
    "v_mfma_f32_16x16x32_bf16 v[80:83], v[120:123], v[60:63], v[80:83]\n"
    "s_setprio 2\n"
    "ds_read_b64_tr_b16 v[56:57], v42 offset:32768\n"
    "ds_read_b64_tr_b16 v[58:59], v42 offset:33792\n"
    "v_fma_f32 v11, -v10, v10, v11\n"
    "v_lshlrev_b32 v12, 16, v12\n"
    "v_mfma_f32_16x16x32_bf16 v[84:87], v[124:127], v[60:63], v[84:87]\n"
    "v_max_f32 v14, 0, v11\n"
    "v_sub_f32 v12, v10, v12\n"
    "s_waitcnt lgkmcnt(8)\n"
    "v_mfma_f32_16x16x32_bf16 v[88:91], v[120:123], v[44:47], v[88:91]\n"
    "ds_read_b64_tr_b16 v[60:61], v43 offset:32768\n"
    "ds_read_b64_tr_b16 v[62:63], v43 offset:33792\n"
    "v_cvt_pk_bf16_f32 v12, v10, v12\n"
    "s_lshl_b64 exec, 1, 63\nbuffer_store_dwordx4 v[12:15], off, %[rw], s42 sc1\ns_mov_b64 exec, -1\ns_nop 0\n"
    "v_mfma_f32_16x16x32_bf16 v[92:95], v[124:127], v[44:47], v[92:95]\n"
    "v_lshrrev_b32 v16, 8, v6\n"
    "v_bfe_u32 v17, v6, 4, 4\n"
    "s_add_u32 m0, s40, 99328\n"
    "s_add_u32 s57, s57, 15360\n"
    "buffer_load_dwordx4 v30, %[rq], s57 offen  lds\n"
    "s_waitcnt lgkmcnt(6)\n"
    "v_mfma_f32_16x16x32_bf16 v[32:35], v[36:39], v[48:51], v[32:35]\n"
    "v_mfma_f32_16x16x32_bf16 v[64:67], v[128:131], v[48:51], v[64:67]\n"
    "s_setprio 1\n"
    "ds_read_b128 v[136:139], v25 offset:16384\n"
    "ds_read_b128 v[140:143], v25 offset:18432\n"
    "ds_read_b64_tr_b16 v[44:45], v40 offset:40960\n"
    "ds_read_b64_tr_b16 v[46:47], v40 offset:41984\n"
    "s_movk_i32 s59, 3456\n"
    "s_mov_b32 s55, 221184\n"
    "v_mfma_f32_16x16x32_bf16 v[68:71], v[132:135], v[48:51], v[68:71]\n"
    "s_lshl_b32 s64, s44, 8\n"
    "s_mul_i32 s65, s43, 81920\n"
    "s_waitcnt lgkmcnt(8)\n"
    "v_mfma_f32_16x16x32_bf16 v[72:75], v[128:131], v[52:55], v[72:75]\n"
    "ds_read_b64_tr_b16 v[48:49], v41 offset:40960\n"
    "ds_read_b64_tr_b16 v[50:51], v41 offset:41984\n"
    "s_cmp_eq_u32 s44, 14\ns_cmovk_i32 s59, 1280\ns_cmovk_i32 s55, 256\ns_cselect_b32 s64, s65, s64\n"
    "s_lshr_b32 s65, s40, 8\n"
    "v_mfma_f32_16x16x32_bf16 v[76:79], v[132:135], v[52:55], v[76:79]\n"
    "s_mul_i32 s65, s65, s59\n"
    "v_and_b32 v18, 0xf0, v30\n"
    "s_add_u32 s56, s56, 1\n"
    "s_cmp_eq_u32 s56, 10\n"
    "s_cselect_b32 s56, 0, s56\n"
    "s_add_u32 m0, s40, 114688\n"
    "s_lshl_b32 s57, s56, 7\n"
    "buffer_load_dwordx4 v28, %[rx], s57 offen  lds\n"
    "s_waitcnt lgkmcnt(8)\n"
    "v_mfma_f32_16x16x32_bf16 v[80:83], v[128:131], v[56:59], v[80:83]\n"
    "s_setprio 0\n"
    "ds_read_b64_tr_b16 v[52:53], v42 offset:40960\n"
    "ds_read_b64_tr_b16 v[54:55], v42 offset:41984\n"
    "s_add_u32 s64, s64, s65\n"
    "v_mad_u32_u24 v23, v16, s59, v18\n"
    "v_mfma_f32_16x16x32_bf16 v[84:87], v[132:135], v[56:59], v[84:87]\n"
    "s_lshl_b32 s59, s59, 2\n"
    "v_add_u32 v23, s64, v23\n"
    "s_waitcnt lgkmcnt(8)\n"
    "v_mfma_f32_16x16x32_bf16 v[88:91], v[128:131], v[60:63], v[88:91]\n"
    "ds_read_b64_tr_b16 v[56:57], v43 offset:40960\n"
    "ds_read_b64_tr_b16 v[58:59], v43 offset:41984\n"
    "s_lshr_b32 s64, s40, 2\n"
    "v_and_b32 v26, 0x1f0, v6\n"
    "v_mfma_f32_16x16x32_bf16 v[92:95], v[132:135], v[60:63], v[92:95]\n"
    "s_and_b32 s64, s64, 0x600\n"
    "v_and_b32 v18, 0x200, v6\n"
    "s_add_u32 m0, s40, 115712\n"
    "s_nop 0\n"
    "buffer_load_dwordx4 v29, %[rx], s57 offen  lds\n"
    "s_waitcnt lgkmcnt(6)\n"
    "v_mfma_f32_16x16x32_bf16 v[32:35], v[36:39], v[44:47], v[32:35]\n"
    "v_mfma_f32_16x16x32_bf16 v[64:67], v[136:139], v[44:47], v[64:67]\n"
    "s_waitcnt vmcnt(7)\n"
    "s_barrier\n"
    "s_setprio 3\n"
    "ds_read_b128 v[144:147], v24 offset:49152\n"
    "ds_read_b128 v[148:151], v24 offset:51200\n"
    "v_add_u32 v40, 0x10000, v40\n"
    "v_add_u32 v41, 0x10000, v41\n"
    "v_add_u32 v42, 0x10000, v42\n"
    "v_add_u32 v43, 0x10000, v43\n"
    "ds_read_b64_tr_b16 v[60:61], v40 offset:0\n"
    "ds_read_b64_tr_b16 v[62:63], v40 offset:1024\n"
    "s_lshl_b32 s65, s43, 11\n"
    "v_lshl_or_b32 v5, v18, 6, v26\n"
    "v_mfma_f32_16x16x32_bf16 v[68:71], v[140:143], v[44:47], v[68:71]\n"
    "s_add_u32 s64, s64, s65\n"
    "s_add_u32 s52, s64, 32768\n"
    "s_waitcnt lgkmcnt(8)\n"
    "v_mfma_f32_16x16x32_bf16 v[72:75], v[136:139], v[48:51], v[72:75]\n"
    "ds_read_b64_tr_b16 v[44:45], v41 offset:0\n"
    "ds_read_b64_tr_b16 v[46:47], v41 offset:1024\n"
    "s_mul_i32 s65, s64, 80\n"
    "s_add_u32 s50, s65, 5287936\n"
    "v_mfma_f32_16x16x32_bf16 v[76:79], v[140:143], v[48:51], v[76:79]\n"
    "s_mov_b32 s49, 1.0\n"
    "s_lshl_b32 s64, s44, 7\n"
    "s_add_u32 m0, s40, 131072\n"
    "s_mul_i32 s57, s56, 245760\n"
    "buffer_load_dwordx4 v30, %[rq], s57 offen  lds\n"
    "s_waitcnt lgkmcnt(8)\n"
    "v_mfma_f32_16x16x32_bf16 v[80:83], v[136:139], v[52:55], v[80:83]\n"
    "s_setprio 2\n"
    "ds_read_b64_tr_b16 v[48:49], v42 offset:0\n"
    "ds_read_b64_tr_b16 v[50:51], v42 offset:1024\n"
    "s_lshl3_add_u32 s64, s43, s64\n"
    "s_add_u32 s51, s64, 1024\n"
    "v_mfma_f32_16x16x32_bf16 v[84:87], v[140:143], v[52:55], v[84:87]\n"
    "s_lshr_b32 s64, s40, 5\n"
    "s_add_u32 s64, s64, 162816\n"
    "s_waitcnt lgkmcnt(8)\n"
    "v_mfma_f32_16x16x32_bf16 v[88:91], v[136:139], v[56:59], v[88:91]\n"
    "ds_read_b64_tr_b16 v[52:53], v43 offset:0\n"
    "ds_read_b64_tr_b16 v[54:55], v43 offset:1024\n"
    "v_lshl_add_u32 v21, v17, 2, s64\n"
    "s_lshr_b32 s64, s40, 6\n"
    "v_mfma_f32_16x16x32_bf16 v[92:95], v[140:143], v[56:59], v[92:95]\n"
    "s_lshl_b32 s65, s44, 8\n"
    "s_add_u32 s64, s64, s65\n"
    "s_add_u32 m0, s40, 132096\n"
    "s_add_u32 s57, s57, 15360\n"
    "buffer_load_dwordx4 v30, %[rq], s57 offen  lds\n"
    "s_waitcnt lgkmcnt(6)\n"
    "v_mfma_f32_16x16x32_bf16 v[32:35], v[36:39], v[60:63], v[32:35]\n"
    "v_mfma_f32_16x16x32_bf16 v[64:67], v[144:147], v[60:63], v[64:67]\n"
    "s_setprio 1\n"
    "ds_read_b128 v[152:155], v25 offset:49152\n"
    "ds_read_b128 v[156:159], v25 offset:51200\n"
    "ds_read_b64_tr_b16 v[56:57], v40 offset:8192\n"
    "ds_read_b64_tr_b16 v[58:59], v40 offset:9216\n"
    "v_lshl_add_u32 v7, v17, 1, s64\n"
    "s_mov_b32 s64, 0x02020202\n"
    "v_mfma_f32_16x16x32_bf16 v[68:71], v[148:151], v[60:63], v[68:71]\n"
    "v_mul_lo_u32 v22, v16, s64\n"
    "s_mul_i32 s64, s45, 20\n"
    "s_waitcnt lgkmcnt(8)\n"
    "v_mfma_f32_16x16x32_bf16 v[72:75], v[144:147], v[44:47], v[72:75]\n"
    "ds_read_b64_tr_b16 v[60:61], v41 offset:8192\n"
    "ds_read_b64_tr_b16 v[62:63], v41 offset:9216\n"
    "v_add_u32 v22, 0x01000100, v22\n"
    "s_add_u32 s65, s64, 19\n"
    "v_mfma_f32_16x16x32_bf16 v[76:79], v[148:151], v[44:47], v[76:79]\n"
    "v_lshrrev_b32 v18, 1, v6\n"
    "s_xor_b32 s65, s65, s64\n"
    "s_add_u32 s56, s56, 1\n"
    "s_cmp_eq_u32 s56, 10\n"
    "s_cselect_b32 s56, 0, s56\n"
    "s_add_u32 m0, s40, 0\n"
    "s_lshl_b32 s57, s56, 7\n"
    "buffer_load_dwordx4 v28, %[rx], s57 offen  lds\n"
    "s_waitcnt lgkmcnt(8)\n"
    "v_mfma_f32_16x16x32_bf16 v[80:83], v[144:147], v[48:51], v[80:83]\n"
    "s_setprio 0\n"
    "ds_read_b64_tr_b16 v[44:45], v42 offset:8192\n"
    "ds_read_b64_tr_b16 v[46:47], v42 offset:9216\n"
    "v_lshrrev_b32 v19, 9, v6\n"
    "s_and_b32 s65, s65, 0x80\n"
    "v_mfma_f32_16x16x32_bf16 v[84:87], v[148:151], v[48:51], v[84:87]\n"
    "v_mul_u32_u24 v19, 640, v19\n"
    "s_or_b32 s65, s65, 0x7f\n"
    "s_waitcnt lgkmcnt(8)\n"
    "v_mfma_f32_16x16x32_bf16 v[88:91], v[144:147], v[52:55], v[88:91]\n"
    "ds_read_b64_tr_b16 v[48:49], v43 offset:8192\n"
    "ds_read_b64_tr_b16 v[50:51], v43 offset:9216\n"
    "v_and_b32 v18, s65, v18\n"
    "s_and_b32 s64, s64, 0xffffff80\n"
    "v_mfma_f32_16x16x32_bf16 v[92:95], v[148:151], v[52:55], v[92:95]\n"
    "v_and_b32 v13, 0x88, v18\n"
    "s_add_u32 s53, s64, 1664\n"
    "s_add_u32 m0, s40, 1024\n"
    "s_nop 0\n"
    "buffer_load_dwordx4 v29, %[rx], s57 offen  lds\n"
    "s_waitcnt lgkmcnt(6)\n"
    "v_mfma_f32_16x16x32_bf16 v[32:35], v[36:39], v[56:59], v[32:35]\n"
    "v_mfma_f32_16x16x32_bf16 v[64:67], v[152:155], v[56:59], v[64:67]\n"
    "s_waitcnt vmcnt(6)\n"
    "s_barrier\n"
    "s_setprio 3\n"
    "v_add_u32 v24, 0x10000, v24\n"
    "v_add_u32 v25, 0x10000, v25\n"
    "ds_read_b128 v[160:163], v24 offset:16384\n"
    "ds_read_b128 v[164:167], v24 offset:18432\n"
    "ds_read_b64_tr_b16 v[52:53], v40 offset:32768\n"
    "ds_read_b64_tr_b16 v[54:55], v40 offset:33792\n"
    "v_add_u32 v12, v18, v19\n"
    "s_lshl4_add_u32 s64, s46, s64\n"
    "v_mfma_f32_16x16x32_bf16 v[68:71], v[156:159], v[56:59], v[68:71]\n"
    "s_add_u32 s54, s64, 1024\n"
    "s_lshr_b32 s64, s40, 4\n"
    "s_waitcnt lgkmcnt(8)\n"
    "v_mfma_f32_16x16x32_bf16 v[72:75], v[152:155], v[60:63], v[72:75]\n"
    "ds_read_b64_tr_b16 v[56:57], v41 offset:32768\n"
    "ds_read_b64_tr_b16 v[58:59], v41 offset:33792\n"
    "v_lshrrev_b32 v18, 2, v26\n"
    "s_add_u32 s64, s64, 148480\n"
    "v_mfma_f32_16x16x32_bf16 v[76:79], v[156:159], v[60:63], v[76:79]\n"
    "v_add_u32 v14, s64, v18\n"
    "v_lshl_add_u32 v15, v16, 4, s64\n"
    "s_add_u32 m0, s40, 16384\n"
    "s_mul_i32 s57, s56, 245760\n"
    "buffer_load_dwordx4 v30, %[rq], s57 offen  lds\n"
    "s_waitcnt lgkmcnt(8)\n"
    "v_mfma_f32_16x16x32_bf16 v[80:83], v[152:155], v[44:47], v[80:83]\n"
    "s_setprio 2\n"
    "ds_read_b64_tr_b16 v[60:61], v42 offset:32768\n"
    "ds_read_b64_tr_b16 v[62:63], v42 offset:33792\n"
    "v_and_b32 v18, 1, v16\n"
    "v_lshrrev_b32 v19, 1, v16\n"
    "v_mfma_f32_16x16x32_bf16 v[84:87], v[156:159], v[44:47], v[84:87]\n"
    "v_lshl_or_b32 v18, v18, 1, v19\n"
    "v_mul_u32_u24 v18, 10240, v18\n"
    "s_waitcnt lgkmcnt(8)\n"
    "v_mfma_f32_16x16x32_bf16 v[88:91], v[152:155], v[48:51], v[88:91]\n"
    "ds_read_b64_tr_b16 v[44:45], v43 offset:32768\n"
    "ds_read_b64_tr_b16 v[46:47], v43 offset:33792\n"
    "v_lshl_add_u32 v0, v7, 3, v18\n"
    "v_xor_b32 v1, 256, v0\n"
    "v_mfma_f32_16x16x32_bf16 v[92:95], v[156:159], v[48:51], v[92:95]\n"
    "v_xor_b32 v2, 512, v0\n"
    "v_xor_b32 v3, 768, v0\n"
    "s_add_u32 m0, s40, 17408\n"
    "s_add_u32 s57, s57, 15360\n"
    "buffer_load_dwordx4 v30, %[rq], s57 offen  lds\n"
    "s_waitcnt lgkmcnt(6)\n"
    "v_mfma_f32_16x16x32_bf16 v[32:35], v[36:39], v[52:55], v[32:35]\n"
    "v_mfma_f32_16x16x32_bf16 v[64:67], v[160:163], v[52:55], v[64:67]\n"
    "s_setprio 1\n"
    "ds_read_b128 v[168:171], v25 offset:16384\n"
    "ds_read_b128 v[172:175], v25 offset:18432\n"
    "ds_read_b64_tr_b16 v[48:49], v40 offset:40960\n"
    "ds_read_b64_tr_b16 v[50:51], v40 offset:41984\n"
    "v_mfma_f32_16x16x32_bf16 v[68:71], v[164:167], v[52:55], v[68:71]\n"
    "s_waitcnt lgkmcnt(8)\n"
    "v_mfma_f32_16x16x32_bf16 v[72:75], v[160:163], v[56:59], v[72:75]\n"
    "ds_read_b64_tr_b16 v[52:53], v41 offset:40960\n"
    "ds_read_b64_tr_b16 v[54:55], v41 offset:41984\n"
    "v_mfma_f32_16x16x32_bf16 v[76:79], v[164:167], v[56:59], v[76:79]\n"
    "s_add_u32 s56, s56, 1\n"
    "s_cmp_eq_u32 s56, 10\n"
    "s_cselect_b32 s56, 0, s56\n"
    "s_add_u32 m0, s40, 32768\n"
    "s_lshl_b32 s57, s56, 7\n"
    "buffer_load_dwordx4 v28, %[rx], s57 offen  lds\n"
    "s_waitcnt lgkmcnt(8)\n"
    "v_mfma_f32_16x16x32_bf16 v[80:83], v[160:163], v[60:63], v[80:83]\n"
    "s_setprio 0\n"
    "ds_read_b64_tr_b16 v[56:57], v42 offset:40960\n"
    "ds_read_b64_tr_b16 v[58:59], v42 offset:41984\n"
    "v_mfma_f32_16x16x32_bf16 v[84:87], v[164:167], v[60:63], v[84:87]\n"
    "s_waitcnt lgkmcnt(8)\n"
    "v_mfma_f32_16x16x32_bf16 v[88:91], v[160:163], v[44:47], v[88:91]\n"
    "ds_read_b64_tr_b16 v[60:61], v43 offset:40960\n"
    "ds_read_b64_tr_b16 v[62:63], v43 offset:41984\n"
    "v_mfma_f32_16x16x32_bf16 v[92:95], v[164:167], v[44:47], v[92:95]\n"
    "s_add_u32 m0, s40, 33792\n"
    "s_nop 0\n"
    "buffer_load_dwordx4 v29, %[rx], s57 offen  lds\n"
    "s_waitcnt lgkmcnt(6)\n"
    "v_mfma_f32_16x16x32_bf16 v[32:35], v[36:39], v[48:51], v[32:35]\n"
    "v_mfma_f32_16x16x32_bf16 v[64:67], v[168:171], v[48:51], v[64:67]\n"
    "s_waitcnt vmcnt(6)\n"
    "s_barrier\n"
    "s_setprio 3\n"
    "ds_read_b128 v[176:179], v24 offset:49152\n"
    "ds_read_b128 v[180:183], v24 offset:51200\n"
    "v_add_u32 v40, 0x10000, v40\n"
    "v_add_u32 v41, 0x10000, v41\n"
    "v_add_u32 v42, 0x10000, v42\n"
    "v_add_u32 v43, 0x10000, v43\n"
    "ds_read_b64_tr_b16 v[44:45], v40 offset:0\n"
    "ds_read_b64_tr_b16 v[46:47], v40 offset:1024\n"
    "v_mfma_f32_16x16x32_bf16 v[68:71], v[172:175], v[48:51], v[68:71]\n"
    "s_waitcnt lgkmcnt(8)\n"
    "v_mfma_f32_16x16x32_bf16 v[72:75], v[168:171], v[52:55], v[72:75]\n"
    "ds_read_b64_tr_b16 v[48:49], v41 offset:0\n"
    "ds_read_b64_tr_b16 v[50:51], v41 offset:1024\n"
    "v_mfma_f32_16x16x32_bf16 v[76:79], v[172:175], v[52:55], v[76:79]\n"
    "s_add_u32 m0, s40, 49152\n"
    "s_mul_i32 s57, s56, 245760\n"
    "buffer_load_dwordx4 v30, %[rq], s57 offen  lds\n"
    "s_waitcnt lgkmcnt(8)\n"
    "v_mfma_f32_16x16x32_bf16 v[80:83], v[168:171], v[56:59], v[80:83]\n"
    "s_setprio 2\n"
    "ds_read_b64_tr_b16 v[52:53], v42 offset:0\n"
    "ds_read_b64_tr_b16 v[54:55], v42 offset:1024\n"
    "v_mfma_f32_16x16x32_bf16 v[84:87], v[172:175], v[56:59], v[84:87]\n"
    "s_waitcnt lgkmcnt(8)\n"
    "v_mfma_f32_16x16x32_bf16 v[88:91], v[168:171], v[60:63], v[88:91]\n"
    "ds_read_b64_tr_b16 v[56:57], v43 offset:0\n"
    "ds_read_b64_tr_b16 v[58:59], v43 offset:1024\n"
    "v_mfma_f32_16x16x32_bf16 v[92:95], v[172:175], v[60:63], v[92:95]\n"
    "s_add_u32 m0, s40, 50176\n"
    "s_add_u32 s57, s57, 15360\n"
    "buffer_load_dwordx4 v30, %[rq], s57 offen  lds\n"
    "s_waitcnt lgkmcnt(6)\n"
    "v_mfma_f32_16x16x32_bf16 v[32:35], v[36:39], v[44:47], v[32:35]\n"
    "v_mfma_f32_16x16x32_bf16 v[64:67], v[176:179], v[44:47], v[64:67]\n"
    "s_setprio 1\n"
    "ds_read_b128 v[184:187], v25 offset:49152\n"
    "ds_read_b128 v[188:191], v25 offset:51200\n"
    "ds_read_b64_tr_b16 v[60:61], v40 offset:8192\n"
    "ds_read_b64_tr_b16 v[62:63], v40 offset:9216\n"
    "v_mfma_f32_16x16x32_bf16 v[68:71], v[180:183], v[44:47], v[68:71]\n"
    "s_waitcnt lgkmcnt(8)\n"
    "v_mfma_f32_16x16x32_bf16 v[72:75], v[176:179], v[48:51], v[72:75]\n"
    "ds_read_b64_tr_b16 v[44:45], v41 offset:8192\n"
    "ds_read_b64_tr_b16 v[46:47], v41 offset:9216\n"
    "v_mfma_f32_16x16x32_bf16 v[76:79], v[180:183], v[48:51], v[76:79]\n"
    "s_add_u32 s56, s56, 1\n"
    "s_cmp_eq_u32 s56, 10\n"
    "s_cselect_b32 s56, 0, s56\n"
    "s_add_u32 m0, s40, 65536\n"
    "s_lshl_b32 s57, s56, 7\n"
    "buffer_load_dwordx4 v28, %[rx], s57 offen  lds\n"
    "s_waitcnt lgkmcnt(8)\n"
    "v_mfma_f32_16x16x32_bf16 v[80:83], v[176:179], v[52:55], v[80:83]\n"
    "s_setprio 0\n"
    "ds_read_b64_tr_b16 v[48:49], v42 offset:8192\n"
    "ds_read_b64_tr_b16 v[50:51], v42 offset:9216\n"
    "v_mfma_f32_16x16x32_bf16 v[84:87], v[180:183], v[52:55], v[84:87]\n"
    "s_waitcnt lgkmcnt(8)\n"
    "v_mfma_f32_16x16x32_bf16 v[88:91], v[176:179], v[56:59], v[88:91]\n"
    "ds_read_b64_tr_b16 v[52:53], v43 offset:8192\n"
    "ds_read_b64_tr_b16 v[54:55], v43 offset:9216\n"
    "v_mfma_f32_16x16x32_bf16 v[92:95], v[180:183], v[56:59], v[92:95]\n"
    "s_add_u32 m0, s40, 66560\n"
    "s_nop 0\n"
    "buffer_load_dwordx4 v29, %[rx], s57 offen  lds\n"
    "s_waitcnt lgkmcnt(6)\n"
    "v_mfma_f32_16x16x32_bf16 v[32:35], v[36:39], v[60:63], v[32:35]\n"
    "v_mfma_f32_16x16x32_bf16 v[64:67], v[184:187], v[60:63], v[64:67]\n"
    "s_waitcnt vmcnt(6)\n"
    "s_barrier\n"
    "s_setprio 3\n"
    "v_add_u32 v24, 0xffff0000, v24\n"
    "v_add_u32 v25, 0xffff0000, v25\n"
    "ds_read_b128 v[192:195], v24 offset:0\n"
    "ds_read_b128 v[196:199], v24 offset:2048\n"
    "v_add_u32 v40, 0xfffe0000, v40\n"
    "v_add_u32 v41, 0xfffe0000, v41\n"
    "v_add_u32 v42, 0xfffe0000, v42\n"
    "v_add_u32 v43, 0xfffe0000, v43\n"
    "ds_read_b64_tr_b16 v[56:57], v40 offset:16384\n"
    "ds_read_b64_tr_b16 v[58:59], v40 offset:17408\n"
    "v_mfma_f32_16x16x32_bf16 v[68:71], v[188:191], v[60:63], v[68:71]\n"
    "s_waitcnt lgkmcnt(8)\n"
    "v_mfma_f32_16x16x32_bf16 v[72:75], v[184:187], v[44:47], v[72:75]\n"
    "ds_read_b64_tr_b16 v[60:61], v41 offset:16384\n"
    "ds_read_b64_tr_b16 v[62:63], v41 offset:17408\n"
    "v_mfma_f32_16x16x32_bf16 v[76:79], v[188:191], v[44:47], v[76:79]\n"
    "s_add_u32 m0, s40, 81920\n"
    "s_mul_i32 s57, s56, 245760\n"
    "buffer_load_dwordx4 v30, %[rq], s57 offen  lds\n"
    "s_waitcnt lgkmcnt(8)\n"
    "v_mfma_f32_16x16x32_bf16 v[80:83], v[184:187], v[48:51], v[80:83]\n"
    "s_setprio 2\n"
    "ds_read_b64_tr_b16 v[44:45], v42 offset:16384\n"
    "ds_read_b64_tr_b16 v[46:47], v42 offset:17408\n"
    "v_mfma_f32_16x16x32_bf16 v[84:87], v[188:191], v[48:51], v[84:87]\n"
    "s_waitcnt lgkmcnt(8)\n"
    "v_mfma_f32_16x16x32_bf16 v[88:91], v[184:187], v[52:55], v[88:91]\n"
    "ds_read_b64_tr_b16 v[48:49], v43 offset:16384\n"
    "ds_read_b64_tr_b16 v[50:51], v43 offset:17408\n"
    "v_mfma_f32_16x16x32_bf16 v[92:95], v[188:191], v[52:55], v[92:95]\n"
    "s_add_u32 m0, s40, 82944\n"
    "s_add_u32 s57, s57, 15360\n"
    "buffer_load_dwordx4 v30, %[rq], s57 offen  lds\n"
    "s_waitcnt lgkmcnt(6)\n"
    "v_mfma_f32_16x16x32_bf16 v[32:35], v[36:39], v[56:59], v[32:35]\n"
    "v_mfma_f32_16x16x32_bf16 v[64:67], v[192:195], v[56:59], v[64:67]\n"
    "s_setprio 1\n"
    "ds_read_b128 v[200:203], v25 offset:0\n"
    "ds_read_b128 v[204:207], v25 offset:2048\n"
    "ds_read_b64_tr_b16 v[52:53], v40 offset:24576\n"
    "ds_read_b64_tr_b16 v[54:55], v40 offset:25600\n"
    "v_mfma_f32_16x16x32_bf16 v[68:71], v[196:199], v[56:59], v[68:71]\n"
    "s_waitcnt lgkmcnt(8)\n"
    "v_mfma_f32_16x16x32_bf16 v[72:75], v[192:195], v[60:63], v[72:75]\n"
    "ds_read_b64_tr_b16 v[56:57], v41 offset:24576\n"
    "ds_read_b64_tr_b16 v[58:59], v41 offset:25600\n"
    "v_mfma_f32_16x16x32_bf16 v[76:79], v[196:199], v[60:63], v[76:79]\n"
    "s_add_u32 s56, s56, 1\n"
    "s_cmp_eq_u32 s56, 10\n"
    "s_cselect_b32 s56, 0, s56\n"
    "s_add_u32 m0, s40, 98304\n"
    "s_lshl_b32 s57, s56, 7\n"
    "buffer_load_dwordx4 v28, %[rx], s57 offen  lds\n"
    "s_waitcnt lgkmcnt(8)\n"
    "v_mfma_f32_16x16x32_bf16 v[80:83], v[192:195], v[44:47], v[80:83]\n"
    "s_setprio 0\n"
    "ds_read_b64_tr_b16 v[60:61], v42 offset:24576\n"
    "ds_read_b64_tr_b16 v[62:63], v42 offset:25600\n"
    "v_mfma_f32_16x16x32_bf16 v[84:87], v[196:199], v[44:47], v[84:87]\n"
    "s_waitcnt lgkmcnt(8)\n"
    "v_mfma_f32_16x16x32_bf16 v[88:91], v[192:195], v[48:51], v[88:91]\n"
    "ds_read_b64_tr_b16 v[44:45], v43 offset:24576\n"
    "ds_read_b64_tr_b16 v[46:47], v43 offset:25600\n"
    "v_mfma_f32_16x16x32_bf16 v[92:95], v[196:199], v[48:51], v[92:95]\n"
    "s_add_u32 m0, s40, 99328\n"
    "s_nop 0\n"
    "buffer_load_dwordx4 v29, %[rx], s57 offen  lds\n"
    "s_waitcnt lgkmcnt(6)\n"
    "v_mfma_f32_16x16x32_bf16 v[32:35], v[36:39], v[52:55], v[32:35]\n"
    "v_mfma_f32_16x16x32_bf16 v[64:67], v[200:203], v[52:55], v[64:67]\n"
    "s_waitcnt vmcnt(6)\n"
    "s_barrier\n"
    "s_setprio 3\n"
    "ds_read_b128 v[208:211], v24 offset:32768\n"
    "ds_read_b128 v[212:215], v24 offset:34816\n"
    "ds_read_b64_tr_b16 v[48:49], v40 offset:49152\n"
    "ds_read_b64_tr_b16 v[50:51], v40 offset:50176\n"
    "v_mfma_f32_16x16x32_bf16 v[68:71], v[204:207], v[52:55], v[68:71]\n"
    "s_waitcnt lgkmcnt(8)\n"
    "v_mfma_f32_16x16x32_bf16 v[72:75], v[200:203], v[56:59], v[72:75]\n"
    "ds_read_b64_tr_b16 v[52:53], v41 offset:49152\n"
    "ds_read_b64_tr_b16 v[54:55], v41 offset:50176\n"
    "v_mfma_f32_16x16x32_bf16 v[76:79], v[204:207], v[56:59], v[76:79]\n"
    "s_add_u32 m0, s40, 114688\n"
    "s_mul_i32 s57, s56, 245760\n"
    "buffer_load_dwordx4 v30, %[rq], s57 offen  lds\n"
    "s_waitcnt lgkmcnt(8)\n"
    "v_mfma_f32_16x16x32_bf16 v[80:83], v[200:203], v[60:63], v[80:83]\n"
    "s_setprio 2\n"
    "ds_read_b64_tr_b16 v[56:57], v42 offset:49152\n"
    "ds_read_b64_tr_b16 v[58:59], v42 offset:50176\n"
    "v_mfma_f32_16x16x32_bf16 v[84:87], v[204:207], v[60:63], v[84:87]\n"
    "s_waitcnt lgkmcnt(8)\n"
    "v_mfma_f32_16x16x32_bf16 v[88:91], v[200:203], v[44:47], v[88:91]\n"
    "ds_read_b64_tr_b16 v[60:61], v43 offset:49152\n"
    "ds_read_b64_tr_b16 v[62:63], v43 offset:50176\n"
    "v_mfma_f32_16x16x32_bf16 v[92:95], v[204:207], v[44:47], v[92:95]\n"
    "s_add_u32 m0, s40, 115712\n"
    "s_add_u32 s57, s57, 15360\n"
    "buffer_load_dwordx4 v30, %[rq], s57 offen  lds\n"
    "s_waitcnt lgkmcnt(6)\n"
    "v_mfma_f32_16x16x32_bf16 v[32:35], v[36:39], v[48:51], v[32:35]\n"
    "v_mfma_f32_16x16x32_bf16 v[64:67], v[208:211], v[48:51], v[64:67]\n"
    "s_setprio 1\n"
    "ds_read_b128 v[216:219], v25 offset:32768\n"
    "ds_read_b128 v[220:223], v25 offset:34816\n"
    "ds_read_b64_tr_b16 v[44:45], v40 offset:57344\n"
    "ds_read_b64_tr_b16 v[46:47], v40 offset:58368\n"
    "v_mfma_f32_16x16x32_bf16 v[68:71], v[212:215], v[48:51], v[68:71]\n"
    "buffer_load_dwordx4 v[28:31], v26, %[rw], s52 offen sc1\n"
    "s_waitcnt lgkmcnt(8)\n"
    "v_mfma_f32_16x16x32_bf16 v[72:75], v[208:211], v[52:55], v[72:75]\n"
    "ds_read_b64_tr_b16 v[48:49], v41 offset:57344\n"
    "ds_read_b64_tr_b16 v[50:51], v41 offset:58368\n"
    "v_mfma_f32_16x16x32_bf16 v[76:79], v[212:215], v[52:55], v[76:79]\n"
    "s_add_u32 s56, s56, 1\n"
    "s_cmp_eq_u32 s56, 10\n"
    "s_cselect_b32 s56, 0, s56\n"
    "s_add_u32 m0, s40, 131072\n"
    "s_mul_i32 s57, s56, s55\n"
    "buffer_load_dwordx4 v23, %[r1], s57 offen  lds\n"
    "s_waitcnt lgkmcnt(8)\n"
    "v_mfma_f32_16x16x32_bf16 v[80:83], v[208:211], v[56:59], v[80:83]\n"
    "s_setprio 0\n"
    "ds_read_b64_tr_b16 v[52:53], v42 offset:57344\n"
    "ds_read_b64_tr_b16 v[54:55], v42 offset:58368\n"
    "v_mfma_f32_16x16x32_bf16 v[84:87], v[212:215], v[56:59], v[84:87]\n"
    "s_waitcnt lgkmcnt(8)\n"
    "v_mfma_f32_16x16x32_bf16 v[88:91], v[208:211], v[60:63], v[88:91]\n"
    "ds_read_b64_tr_b16 v[56:57], v43 offset:57344\n"
    "ds_read_b64_tr_b16 v[58:59], v43 offset:58368\n"
    "v_mfma_f32_16x16x32_bf16 v[92:95], v[212:215], v[60:63], v[92:95]\n"
    "s_add_u32 m0, s40, 132096\n"
    "s_add_u32 s57, s57, s59\n"
    "buffer_load_dwordx4 v23, %[r1], s57 offen  lds\n"
    "s_waitcnt lgkmcnt(6)\n"
    "v_mfma_f32_16x16x32_bf16 v[32:35], v[36:39], v[44:47], v[32:35]\n"
    "v_mfma_f32_16x16x32_bf16 v[64:67], v[216:219], v[44:47], v[64:67]\n"
    "s_waitcnt vmcnt(7)\n"
    "s_barrier\n"
    "s_setprio 3\n"
    "v_add_u32 v24, 0x10000, v24\n"
    "v_add_u32 v25, 0x10000, v25\n"
    "ds_read_b128 v[224:227], v24 offset:0\n"
    "ds_read_b128 v[228:231], v24 offset:2048\n"
    "v_add_u32 v40, 0x10000, v40\n"
    "v_add_u32 v41, 0x10000, v41\n"
    "v_add_u32 v42, 0x10000, v42\n"
    "v_add_u32 v43, 0x10000, v43\n"
    "ds_read_b64_tr_b16 v[60:61], v40 offset:16384\n"
    "ds_read_b64_tr_b16 v[62:63], v40 offset:17408\n"
    "v_mfma_f32_16x16x32_bf16 v[68:71], v[220:223], v[44:47], v[68:71]\n"
    "s_waitcnt lgkmcnt(8)\n"
    "v_mfma_f32_16x16x32_bf16 v[72:75], v[216:219], v[48:51], v[72:75]\n"
    "ds_read_b64_tr_b16 v[44:45], v41 offset:16384\n"
    "ds_read_b64_tr_b16 v[46:47], v41 offset:17408\n"
    "v_mfma_f32_16x16x32_bf16 v[76:79], v[220:223], v[48:51], v[76:79]\n"
    "s_add_u32 s56, s56, 1\n"
    "s_cmp_eq_u32 s56, 10\n"
    "s_cselect_b32 s56, 0, s56\n"
    "s_add_u32 m0, s40, 0\n"
    "s_mul_i32 s57, s56, s55\n"
    "buffer_load_dwordx4 v23, %[r1], s57 offen  lds\n"
    "s_waitcnt lgkmcnt(8)\n"
    "v_mfma_f32_16x16x32_bf16 v[80:83], v[216:219], v[52:55], v[80:83]\n"
    "s_setprio 2\n"
    "ds_read_b64_tr_b16 v[48:49], v42 offset:16384\n"
    "ds_read_b64_tr_b16 v[50:51], v42 offset:17408\n"
    "v_mfma_f32_16x16x32_bf16 v[84:87], v[220:223], v[52:55], v[84:87]\n"
    "s_waitcnt lgkmcnt(8)\n"
    "v_mfma_f32_16x16x32_bf16 v[88:91], v[216:219], v[56:59], v[88:91]\n"
    "ds_read_b64_tr_b16 v[52:53], v43 offset:16384\n"
    "ds_read_b64_tr_b16 v[54:55], v43 offset:17408\n"
    "v_mfma_f32_16x16x32_bf16 v[92:95], v[220:223], v[56:59], v[92:95]\n"
    "s_add_u32 m0, s40, 1024\n"
    "s_add_u32 s57, s57, s59\n"
    "buffer_load_dwordx4 v23, %[r1], s57 offen  lds\n"
    "s_waitcnt lgkmcnt(6)\n"
    "v_mfma_f32_16x16x32_bf16 v[32:35], v[36:39], v[60:63], v[32:35]\n"
    "v_mfma_f32_16x16x32_bf16 v[64:67], v[224:227], v[60:63], v[64:67]\n"
    "s_setprio 1\n"
    "ds_read_b128 v[232:235], v25 offset:0\n"
    "ds_read_b128 v[236:239], v25 offset:2048\n"
    "ds_read_b64_tr_b16 v[56:57], v40 offset:24576\n"
    "ds_read_b64_tr_b16 v[58:59], v40 offset:25600\n"
    "v_mfma_f32_16x16x32_bf16 v[68:71], v[228:231], v[60:63], v[68:71]\n"
    "s_waitcnt lgkmcnt(8)\n"
    "v_mfma_f32_16x16x32_bf16 v[72:75], v[224:227], v[44:47], v[72:75]\n"
    "ds_read_b64_tr_b16 v[60:61], v41 offset:24576\n"
    "ds_read_b64_tr_b16 v[62:63], v41 offset:25600\n"
    "v_mfma_f32_16x16x32_bf16 v[76:79], v[228:231], v[44:47], v[76:79]\n"
    "s_add_u32 s56, s56, 1\n"
    "s_cmp_eq_u32 s56, 10\n"
    "s_cselect_b32 s56, 0, s56\n"
    "s_add_u32 m0, s40, 16384\n"
    "s_mul_i32 s57, s56, s55\n"
    "buffer_load_dwordx4 v23, %[r1], s57 offen  lds\n"
    "s_waitcnt lgkmcnt(8)\n"
    "v_mfma_f32_16x16x32_bf16 v[80:83], v[224:227], v[48:51], v[80:83]\n"
    "s_setprio 0\n"
    "ds_read_b64_tr_b16 v[44:45], v42 offset:24576\n"
    "ds_read_b64_tr_b16 v[46:47], v42 offset:25600\n"
    "v_mfma_f32_16x16x32_bf16 v[84:87], v[228:231], v[48:51], v[84:87]\n"
    "s_waitcnt lgkmcnt(8)\n"
    "v_mfma_f32_16x16x32_bf16 v[88:91], v[224:227], v[52:55], v[88:91]\n"
    "ds_read_b64_tr_b16 v[48:49], v43 offset:24576\n"
    "ds_read_b64_tr_b16 v[50:51], v43 offset:25600\n"
    "v_mfma_f32_16x16x32_bf16 v[92:95], v[228:231], v[52:55], v[92:95]\n"
    "s_add_u32 m0, s40, 17408\n"
    "s_add_u32 s57, s57, s59\n"
    "buffer_load_dwordx4 v23, %[r1], s57 offen  lds\n"
    "s_waitcnt lgkmcnt(6)\n"
    "v_mfma_f32_16x16x32_bf16 v[32:35], v[36:39], v[56:59], v[32:35]\n"
    "v_mfma_f32_16x16x32_bf16 v[64:67], v[232:235], v[56:59], v[64:67]\n"
    "s_waitcnt vmcnt(7)\n"
    "s_barrier\n"
    "s_setprio 3\n"
    "ds_read_b128 v[240:243], v24 offset:32768\n"
    "ds_read_b128 v[244:247], v24 offset:34816\n"
    "ds_read_b64_tr_b16 v[52:53], v40 offset:49152\n"
    "ds_read_b64_tr_b16 v[54:55], v40 offset:50176\n"
    "v_mfma_f32_16x16x32_bf16 v[68:71], v[236:239], v[56:59], v[68:71]\n"
    "s_waitcnt lgkmcnt(8)\n"
    "v_mfma_f32_16x16x32_bf16 v[72:75], v[232:235], v[60:63], v[72:75]\n"
    "ds_read_b128 v[248:251], v25 offset:32768\n"
    "ds_read_b128 v[252:255], v25 offset:34816\n"
    "ds_read_b64_tr_b16 v[56:57], v40 offset:57344\n"
    "ds_read_b64_tr_b16 v[58:59], v40 offset:58368\n"
    "v_mfma_f32_16x16x32_bf16 v[76:79], v[236:239], v[60:63], v[76:79]\n"
    "s_add_u32 s56, s56, 1\n"
    "s_cmp_eq_u32 s56, 10\n"
    "s_cselect_b32 s56, 0, s56\n"
    "s_add_u32 m0, s40, 32768\n"
    "s_mul_i32 s57, s56, s55\n"
    "buffer_load_dwordx4 v23, %[r1], s57 offen  lds\n"
    "s_waitcnt lgkmcnt(10)\n"
    "v_mfma_f32_16x16x32_bf16 v[80:83], v[232:235], v[44:47], v[80:83]\n"
    "s_setprio 2\n"
    "ds_read_b64_tr_b16 v[60:61], v41 offset:49152\n"
    "ds_read_b64_tr_b16 v[62:63], v41 offset:50176\n"
    "v_mfma_f32_16x16x32_bf16 v[84:87], v[236:239], v[44:47], v[84:87]\n"
    "s_waitcnt vmcnt(7)\n"
    "s_mov_b32 s58, 1048576\n"
    "3:\n"
    "v_cmp_ne_u32 s[62:63], s60, v29\n"
    "s_mov_b64 vcc, s[62:63]\n"
    "v_cmp_ne_u32 s[62:63], s61, v31\n"
    "s_or_b64 vcc, vcc, s[62:63]\n"
    "s_cbranch_scc0 4f\n"
    "s_sub_u32 s58, s58, 1\n"
    "s_cbranch_scc1 4f\n"
    "s_sleep 2\n"
    "buffer_load_dwordx4 v[28:31], v26, %[rw], s52 offen sc1\n"
    "s_waitcnt vmcnt(0)\n"
    "s_branch 3b\n"
    "4:\n"
    "s_waitcnt lgkmcnt(10)\n"
    "v_mfma_f32_16x16x32_bf16 v[88:91], v[232:235], v[48:51], v[88:91]\n"
    "ds_read_b64_tr_b16 v[44:45], v42 offset:49152\n"
    "ds_read_b64_tr_b16 v[46:47], v42 offset:50176\n"
    "v_add_f32 v27, 0x3727c5ac, v30\n"
    "v_rsq_f32 v27, v27\n"
    "v_mfma_f32_16x16x32_bf16 v[92:95], v[236:239], v[48:51], v[92:95]\n"
    "v_mov_b32 v24, v28\n"
    "v_mul_f32 v29, v30, v27\n"
    "s_add_u32 m0, s40, 33792\n"
    "s_add_u32 s57, s57, s59\n"
    "buffer_load_dwordx4 v23, %[r1], s57 offen  lds\n"
    "s_waitcnt lgkmcnt(8)\n"
    "v_mfma_f32_16x16x32_bf16 v[32:35], v[36:39], v[52:55], v[32:35]\n"
    "v_mfma_f32_16x16x32_bf16 v[64:67], v[240:243], v[52:55], v[64:67]\n"
    "s_setprio 1\n"
    "ds_read_b64_tr_b16 v[48:49], v43 offset:49152\n"
    "ds_read_b64_tr_b16 v[50:51], v43 offset:50176\n"
    "v_fmaak_f32 v29, v29, v27, 0x3727c5ac\n"
    "v_rsq_f32 v29, v29\n"
    "v_mfma_f32_16x16x32_bf16 v[68:71], v[244:247], v[52:55], v[68:71]\n"
    "v_mul_f32 v30, s49, v27\n"
    "v_mul_f32 v31, v29, v27\n"
    "v_rcp_f32 v29, v31\n"
    "s_waitcnt lgkmcnt(6)\n"
    "v_mfma_f32_16x16x32_bf16 v[32:35], v[36:39], v[56:59], v[32:35]\n"
    "v_mfma_f32_16x16x32_bf16 v[64:67], v[248:251], v[56:59], v[64:67]\n"
    "ds_read_b64_tr_b16 v[52:53], v41 offset:57344\n"
    "ds_read_b64_tr_b16 v[54:55], v41 offset:58368\n"
    "v_mov_b32 v26, v30\n"
    "v_mov_b32 v8, v27\n"
    "v_mfma_f32_16x16x32_bf16 v[68:71], v[252:255], v[56:59], v[68:71]\n"
    "v_cvt_pk_bf16_f32 v27, v29, v29\n"
    "v_lshlrev_b32 v27, 16, v27\n"
    "s_add_u32 s56, s56, 1\n"
    "s_cmp_eq_u32 s56, 10\n"
    "s_cselect_b32 s56, 0, s56\n"
    "s_add_u32 m0, s40, 49152\n"
    "s_mul_i32 s57, s56, s55\n"
    "buffer_load_dwordx4 v23, %[r1], s57 offen  lds\n"
    "s_waitcnt lgkmcnt(6)\n"
    "v_mfma_f32_16x16x32_bf16 v[72:75], v[240:243], v[60:63], v[72:75]\n"
    "s_setprio 0\n"
    "ds_read_b64_tr_b16 v[56:57], v42 offset:57344\n"
    "ds_read_b64_tr_b16 v[58:59], v42 offset:58368\n"
    "v_sub_f32 v27, v29, v27\n"
    "v_cvt_pk_bf16_f32 v29, v29, v27\n"
    "v_mfma_f32_16x16x32_bf16 v[76:79], v[244:247], v[60:63], v[76:79]\n"
    "v_mov_b32 v27, v31\n"
    "v_mov_b32 v25, v29\n"
    "s_nop 1\n"
    "v_cvt_pk_bf16_f32 v33, v32, v32\n"
    "v_lshlrev_b32 v33, 16, v33\n"
    "v_sub_f32 v33, v32, v33\n"
    "v_cvt_pk_bf16_f32 v32, v32, v33\n"
    "ds_write_b32 v21, v32\n"
    "ds_write_b32 v14, v8\n"
    "s_waitcnt lgkmcnt(8)\n"
    "v_mfma_f32_16x16x32_bf16 v[80:83], v[240:243], v[44:47], v[80:83]\n"
    "ds_read_b64_tr_b16 v[60:61], v43 offset:57344\n"
    "ds_read_b64_tr_b16 v[62:63], v43 offset:58368\n"
    "v_permlane16_swap_b32 v28, v24\n"
    "v_permlane16_swap_b32 v30, v26\n"
    "v_mfma_f32_16x16x32_bf16 v[84:87], v[244:247], v[44:47], v[84:87]\n"
    "v_permlane16_swap_b32 v31, v27\n"
    "v_permlane16_swap_b32 v29, v25\n"
    "s_add_u32 m0, s40, 50176\n"
    "s_add_u32 s57, s57, s59\n"
    "buffer_load_dwordx4 v23, %[r1], s57 offen  lds\n"
    "s_waitcnt lgkmcnt(8)\n"
    "v_mfma_f32_16x16x32_bf16 v[88:91], v[240:243], v[48:51], v[88:91]\n"
    "v_perm_b32 v28, 0, v28, v22\n"
    "v_perm_b32 v24, 0, v24, v22\n"
    "v_mfma_f32_16x16x32_bf16 v[92:95], v[244:247], v[48:51], v[92:95]\n"
    "v_perm_b32 v29, 0, v29, v22\n"
    "v_perm_b32 v25, 0, v25, v22\n"
    "s_waitcnt vmcnt(8) lgkmcnt(0)\n"
    "s_barrier\n"
    "v_xor_b32 v32, 0, v21\n"
    "v_xor_b32 v33, 64, v21\n"
    "v_xor_b32 v34, 128, v21\n"
    "v_xor_b32 v35, 192, v21\n"
    "ds_read_b32 v44, v32\n"
    "ds_read_b32 v46, v33\n"
    "ds_read_b32 v48, v34\n"
    "ds_read_b32 v50, v35\n"
    "ds_read_b128 v[32:35], v15\n"
    "ds_read_b128 v[8:11], v15 offset:64\n"
    "s_add_u32 s56, s56, 1\n"
    "s_cmp_eq_u32 s56, 10\n"
    "s_cselect_b32 s56, 0, s56\n"
    "s_add_u32 m0, s40, 65536\n"
    "s_mul_i32 s57, s56, s55\n"
    "buffer_load_dwordx4 v23, %[r1], s57 offen  lds\n"
    "v_mfma_f32_16x16x32_bf16 v[72:75], v[248:251], v[52:55], v[72:75]\n"
    "v_mov_b32 v45, 0\n"
    "v_mov_b32 v47, 0\n"
    "v_mfma_f32_16x16x32_bf16 v[76:79], v[252:255], v[52:55], v[76:79]\n"
    "v_mov_b32 v49, 0\n"
    "v_mov_b32 v51, 0\n"
    "s_add_u32 m0, s40, 66560\n"
    "s_add_u32 s57, s57, s59\n"
    "buffer_load_dwordx4 v23, %[r1], s57 offen  lds\n"
    "v_mfma_f32_16x16x32_bf16 v[80:83], v[248:251], v[56:59], v[80:83]\n"
    "v_add_u32 v40, 0x10000, v40\n"
    "v_add_u32 v41, 0x10000, v41\n"
    "v_add_u32 v42, 0x10000, v42\n"
    "v_add_u32 v43, 0x10000, v43\n"
    "ds_read_b64_tr_b16 v[52:53], v40 offset:0\n"
    "ds_read_b64_tr_b16 v[54:55], v40 offset:1024\n"
    "v_mfma_f32_16x16x32_bf16 v[84:87], v[252:255], v[56:59], v[84:87]\n"
    "s_add_u32 s56, s56, 1\n"
    "s_cmp_eq_u32 s56, 10\n"
    "s_cselect_b32 s56, 0, s56\n"
    "s_add_u32 m0, s40, 81920\n"
    "s_mul_i32 s57, s56, s55\n"
    "buffer_load_dwordx4 v23, %[r1], s57 offen  lds\n"
    "v_mfma_f32_16x16x32_bf16 v[88:91], v[248:251], v[60:63], v[88:91]\n"
    "ds_read_b64_tr_b16 v[56:57], v41 offset:0\n"
    "ds_read_b64_tr_b16 v[58:59], v41 offset:1024\n"
    "v_mfma_f32_16x16x32_bf16 v[92:95], v[252:255], v[60:63], v[92:95]\n"
    "s_add_u32 m0, s40, 82944\n"
    "s_add_u32 s57, s57, s59\n"
    "buffer_load_dwordx4 v23, %[r1], s57 offen  lds\n"
    "s_waitcnt lgkmcnt(4)\n"
    "v_mfma_f32_16x16x16_bf16 v[64:67], v[28:29], v[44:45], v[64:67]\n"
    "ds_read_b64_tr_b16 v[60:61], v42 offset:0\n"
    "ds_read_b64_tr_b16 v[62:63], v42 offset:1024\n"
    "v_mfma_f32_16x16x16_bf16 v[68:71], v[24:25], v[44:45], v[68:71]\n"
    "v_mfma_f32_16x16x16_bf16 v[72:75], v[28:29], v[46:47], v[72:75]\n"
    "v_mfma_f32_16x16x16_bf16 v[76:79], v[24:25], v[46:47], v[76:79]\n"
    "v_mfma_f32_16x16x16_bf16 v[80:83], v[28:29], v[48:49], v[80:83]\n"
    "v_mfma_f32_16x16x16_bf16 v[84:87], v[24:25], v[48:49], v[84:87]\n"
    "v_mfma_f32_16x16x16_bf16 v[88:91], v[28:29], v[50:51], v[88:91]\n"
    "v_mfma_f32_16x16x16_bf16 v[92:95], v[24:25], v[50:51], v[92:95]\n"
    "ds_read_b64_tr_b16 v[44:45], v43 offset:0\n"
    "ds_read_b64_tr_b16 v[46:47], v43 offset:1024\n"
    "v_mul_f32 v64, v32, v64\n"
    "v_mul_f32 v65, v33, v65\n"
    "v_mul_f32 v66, v34, v66\n"
    "v_mul_f32 v67, v35, v67\n"
    "v_mul_f32 v68, v8, v68\n"
    "v_mul_f32 v69, v9, v69\n"
    "v_mul_f32 v70, v10, v70\n"
    "v_mul_f32 v71, v11, v71\n"
    "v_cvt_pk_bf16_f32 v64, v64, v65\n"
    "v_cvt_pk_bf16_f32 v65, v66, v67\n"
    "v_cvt_pk_bf16_f32 v66, v68, v69\n"
    "v_cvt_pk_bf16_f32 v67, v70, v71\n"
    "v_mul_f32 v72, v32, v72\n"
    "v_mul_f32 v73, v33, v73\n"
    "v_mul_f32 v74, v34, v74\n"
    "v_mul_f32 v75, v35, v75\n"
    "v_permlane16_swap_b32 v64, v66\n"
    "v_permlane16_swap_b32 v65, v67\n"
    "buffer_store_dwordx4 v[64:67], v0, %[rw], s50 offen sc1\n"
    "v_mul_f32 v76, v8, v76\n"
    "v_mul_f32 v77, v9, v77\n"
    "v_mul_f32 v78, v10, v78\n"
    "v_mul_f32 v79, v11, v79\n"
    "v_cvt_pk_bf16_f32 v72, v72, v73\n"
    "v_cvt_pk_bf16_f32 v73, v74, v75\n"
    "v_cvt_pk_bf16_f32 v74, v76, v77\n"
    "v_cvt_pk_bf16_f32 v75, v78, v79\n"
    "v_mul_f32 v80, v32, v80\n"
    "v_mul_f32 v81, v33, v81\n"
    "v_mul_f32 v82, v34, v82\n"
    "v_mul_f32 v83, v35, v83\n"
    "v_permlane16_swap_b32 v72, v74\n"
    "v_permlane16_swap_b32 v73, v75\n"
    "buffer_store_dwordx4 v[72:75], v1, %[rw], s50 offen sc1\n"
    "v_mul_f32 v84, v8, v84\n"
    "v_mul_f32 v85, v9, v85\n"
    "v_mul_f32 v86, v10, v86\n"
    "v_mul_f32 v87, v11, v87\n"
    "v_cvt_pk_bf16_f32 v16, v80, v81\n"
    "v_cvt_pk_bf16_f32 v17, v82, v83\n"
    "v_cvt_pk_bf16_f32 v18, v84, v85\n"
    "v_cvt_pk_bf16_f32 v19, v86, v87\n"
    "v_mul_f32 v88, v32, v88\n"
    "v_mul_f32 v89, v33, v89\n"
    "v_mul_f32 v90, v34, v90\n"
    "v_mul_f32 v91, v35, v91\n"
    "v_permlane16_swap_b32 v16, v18\n"
    "v_permlane16_swap_b32 v17, v19\n"
    "v_mul_f32 v92, v8, v92\n"
    "v_mul_f32 v93, v9, v93\n"
    "v_mul_f32 v94, v10, v94\n"
    "v_mul_f32 v95, v11, v95\n"
    "v_cvt_pk_bf16_f32 v8, v88, v89\n"
    "v_cvt_pk_bf16_f32 v9, v90, v91\n"
    "v_cvt_pk_bf16_f32 v10, v92, v93\n"
    "v_cvt_pk_bf16_f32 v11, v94, v95\n"
    "s_nop 1\n"
    "v_permlane16_swap_b32 v8, v10\n"
    "v_permlane16_swap_b32 v9, v11\n"
    ".Lp1_h:\n"
    "s_cmp_eq_u32 s44, 14\n"
    "s_cbranch_scc1 .Lp1_x\n"
    "s_waitcnt lgkmcnt(6)\n"
    "v_mfma_f32_16x16x32_bf16 v[32:35], v[36:39], v[52:55], 0\n"
    "v_mfma_f32_16x16x32_bf16 v[64:67], v[52:55], v[96:99], 0\n"
    "s_setprio 1\n"
    "ds_read_b64_tr_b16 v[48:49], v40 offset:8192\n"
    "ds_read_b64_tr_b16 v[50:51], v40 offset:9216\n"
    "v_mfma_f32_16x16x32_bf16 v[68:71], v[52:55], v[100:103], 0\n"
    "s_waitcnt lgkmcnt(6)\n"
    "v_mfma_f32_16x16x32_bf16 v[72:75], v[56:59], v[96:99], 0\n"
    "ds_read_b64_tr_b16 v[52:53], v41 offset:8192\n"
    "ds_read_b64_tr_b16 v[54:55], v41 offset:9216\n"
    "v_mfma_f32_16x16x32_bf16 v[76:79], v[56:59], v[100:103], 0\n"
    "s_waitcnt lgkmcnt(6)\n"
    "v_mfma_f32_16x16x32_bf16 v[80:83], v[60:63], v[96:99], 0\n"
    "s_setprio 0\n"
    "ds_read_b64_tr_b16 v[56:57], v42 offset:8192\n"
    "ds_read_b64_tr_b16 v[58:59], v42 offset:9216\n"
    "v_mfma_f32_16x16x32_bf16 v[84:87], v[60:63], v[100:103], 0\n"
    "s_waitcnt lgkmcnt(6)\n"
    "v_mfma_f32_16x16x32_bf16 v[88:91], v[44:47], v[96:99], 0\n"
    "ds_read_b64_tr_b16 v[60:61], v43 offset:8192\n"
    "ds_read_b64_tr_b16 v[62:63], v43 offset:9216\n"
    "v_mfma_f32_16x16x32_bf16 v[92:95], v[44:47], v[100:103], 0\n"
    "s_waitcnt lgkmcnt(6)\n"
    "v_mfma_f32_16x16x32_bf16 v[32:35], v[36:39], v[48:51], v[32:35]\n"
    "v_mfma_f32_16x16x32_bf16 v[64:67], v[48:51], v[104:107], v[64:67]\n"
    "s_waitcnt vmcnt(12)\n"
    "s_barrier\n"
    "s_setprio 3\n"
    "v_add_u32 v40, 0xfffe0000, v40\n"
    "v_add_u32 v41, 0xfffe0000, v41\n"
    "v_add_u32 v42, 0xfffe0000, v42\n"
    "v_add_u32 v43, 0xfffe0000, v43\n"
    "ds_read_b64_tr_b16 v[44:45], v40 offset:0\n"
    "ds_read_b64_tr_b16 v[46:47], v40 offset:1024\n"
    "v_mfma_f32_16x16x32_bf16 v[68:71], v[48:51], v[108:111], v[68:71]\n"
    "s_waitcnt lgkmcnt(6)\n"
    "v_mfma_f32_16x16x32_bf16 v[72:75], v[52:55], v[104:107], v[72:75]\n"
    "ds_read_b64_tr_b16 v[48:49], v41 offset:0\n"
    "ds_read_b64_tr_b16 v[50:51], v41 offset:1024\n"
    "v_mfma_f32_16x16x32_bf16 v[76:79], v[52:55], v[108:111], v[76:79]\n"
    "s_waitcnt lgkmcnt(6)\n"
    "v_mfma_f32_16x16x32_bf16 v[80:83], v[56:59], v[104:107], v[80:83]\n"
    "s_setprio 2\n"
    "ds_read_b64_tr_b16 v[52:53], v42 offset:0\n"
    "ds_read_b64_tr_b16 v[54:55], v42 offset:1024\n"
    "v_mfma_f32_16x16x32_bf16 v[84:87], v[56:59], v[108:111], v[84:87]\n"
    "s_waitcnt lgkmcnt(6)\n"
    "v_mfma_f32_16x16x32_bf16 v[88:91], v[60:63], v[104:107], v[88:91]\n"
    "ds_read_b64_tr_b16 v[56:57], v43 offset:0\n"
    "ds_read_b64_tr_b16 v[58:59], v43 offset:1024\n"
    "v_mfma_f32_16x16x32_bf16 v[92:95], v[60:63], v[108:111], v[92:95]\n"
    "s_waitcnt lgkmcnt(6)\n"
    "v_mfma_f32_16x16x32_bf16 v[32:35], v[36:39], v[44:47], v[32:35]\n"
    "v_mfma_f32_16x16x32_bf16 v[64:67], v[44:47], v[112:115], v[64:67]\n"
    "s_setprio 1\n"
    "ds_read_b64_tr_b16 v[60:61], v40 offset:8192\n"
    "ds_read_b64_tr_b16 v[62:63], v40 offset:9216\n"
    "v_mfma_f32_16x16x32_bf16 v[68:71], v[44:47], v[116:119], v[68:71]\n"
    "s_waitcnt lgkmcnt(6)\n"
    "v_mfma_f32_16x16x32_bf16 v[72:75], v[48:51], v[112:115], v[72:75]\n"
    "ds_read_b64_tr_b16 v[44:45], v41 offset:8192\n"
    "ds_read_b64_tr_b16 v[46:47], v41 offset:9216\n"
    "v_mfma_f32_16x16x32_bf16 v[76:79], v[48:51], v[116:119], v[76:79]\n"
    "s_waitcnt lgkmcnt(6)\n"
    "v_mfma_f32_16x16x32_bf16 v[80:83], v[52:55], v[112:115], v[80:83]\n"
    "s_setprio 0\n"
    "ds_read_b64_tr_b16 v[48:49], v42 offset:8192\n"
    "ds_read_b64_tr_b16 v[50:51], v42 offset:9216\n"
    "v_mfma_f32_16x16x32_bf16 v[84:87], v[52:55], v[116:119], v[84:87]\n"
    "buffer_store_dwordx4 v[16:19], v2, %[rw], s50 offen sc1\n"
    "s_waitcnt lgkmcnt(6)\n"
    "v_mfma_f32_16x16x32_bf16 v[88:91], v[56:59], v[112:115], v[88:91]\n"
    "ds_read_b64_tr_b16 v[52:53], v43 offset:8192\n"
    "ds_read_b64_tr_b16 v[54:55], v43 offset:9216\n"
    "v_mfma_f32_16x16x32_bf16 v[92:95], v[56:59], v[116:119], v[92:95]\n"
    "s_waitcnt lgkmcnt(6)\n"
    "v_mfma_f32_16x16x32_bf16 v[32:35], v[36:39], v[60:63], v[32:35]\n"
    "v_mfma_f32_16x16x32_bf16 v[64:67], v[60:63], v[120:123], v[64:67]\n"
    "s_waitcnt vmcnt(11)\n"
    "s_barrier\n"
    "s_setprio 3\n"
    "ds_read_b64_tr_b16 v[56:57], v40 offset:16384\n"
    "ds_read_b64_tr_b16 v[58:59], v40 offset:17408\n"
    "v_mfma_f32_16x16x32_bf16 v[68:71], v[60:63], v[124:127], v[68:71]\n"
    "s_waitcnt lgkmcnt(6)\n"
    "v_mfma_f32_16x16x32_bf16 v[72:75], v[44:47], v[120:123], v[72:75]\n"
    "ds_read_b64_tr_b16 v[60:61], v41 offset:16384\n"
    "ds_read_b64_tr_b16 v[62:63], v41 offset:17408\n"
    "v_mfma_f32_16x16x32_bf16 v[76:79], v[44:47], v[124:127], v[76:79]\n"
    "s_waitcnt lgkmcnt(6)\n"
    "v_mfma_f32_16x16x32_bf16 v[80:83], v[48:51], v[120:123], v[80:83]\n"
    "s_setprio 2\n"
    "ds_read_b64_tr_b16 v[44:45], v42 offset:16384\n"
    "ds_read_b64_tr_b16 v[46:47], v42 offset:17408\n"
    "v_mfma_f32_16x16x32_bf16 v[84:87], v[48:51], v[124:127], v[84:87]\n"
    "s_waitcnt lgkmcnt(6)\n"
    "v_mfma_f32_16x16x32_bf16 v[88:91], v[52:55], v[120:123], v[88:91]\n"
    "ds_read_b64_tr_b16 v[48:49], v43 offset:16384\n"
    "ds_read_b64_tr_b16 v[50:51], v43 offset:17408\n"
    "v_mfma_f32_16x16x32_bf16 v[92:95], v[52:55], v[124:127], v[92:95]\n"
    "s_waitcnt lgkmcnt(6)\n"
    "v_mfma_f32_16x16x32_bf16 v[32:35], v[36:39], v[56:59], v[32:35]\n"
    "v_mfma_f32_16x16x32_bf16 v[64:67], v[56:59], v[128:131], v[64:67]\n"
    "s_setprio 1\n"
    "ds_read_b64_tr_b16 v[52:53], v40 offset:24576\n"
    "ds_read_b64_tr_b16 v[54:55], v40 offset:25600\n"
    "v_mfma_f32_16x16x32_bf16 v[68:71], v[56:59], v[132:135], v[68:71]\n"
    "s_waitcnt lgkmcnt(6)\n"
    "v_mfma_f32_16x16x32_bf16 v[72:75], v[60:63], v[128:131], v[72:75]\n"
    "ds_read_b64_tr_b16 v[56:57], v41 offset:24576\n"
    "ds_read_b64_tr_b16 v[58:59], v41 offset:25600\n"
    "v_mfma_f32_16x16x32_bf16 v[76:79], v[60:63], v[132:135], v[76:79]\n"
    "s_waitcnt lgkmcnt(6)\n"
    "v_mfma_f32_16x16x32_bf16 v[80:83], v[44:47], v[128:131], v[80:83]\n"
    "s_setprio 0\n"
    "ds_read_b64_tr_b16 v[60:61], v42 offset:24576\n"
    "ds_read_b64_tr_b16 v[62:63], v42 offset:25600\n"
    "v_mfma_f32_16x16x32_bf16 v[84:87], v[44:47], v[132:135], v[84:87]\n"
    "v_lshrrev_b32 v120, 2, v6\n"
    "v_mov_b32 v122, 0\n"
    "v_add_u32 v120, 163328, v120\n"
    "v_mov_b32 v121, 1\n"
    "ds_write_b32 v120, v122\n"
    "s_waitcnt lgkmcnt(7)\n"
    "v_mfma_f32_16x16x32_bf16 v[88:91], v[48:51], v[128:131], v[88:91]\n"
    "ds_read_b64_tr_b16 v[44:45], v43 offset:24576\n"
    "ds_read_b64_tr_b16 v[46:47], v43 offset:25600\n"
    "v_mfma_f32_16x16x32_bf16 v[92:95], v[48:51], v[132:135], v[92:95]\n"
    "s_waitcnt lgkmcnt(7)\n"
    "v_mfma_f32_16x16x32_bf16 v[32:35], v[36:39], v[52:55], v[32:35]\n"
    "v_mfma_f32_16x16x32_bf16 v[64:67], v[52:55], v[136:139], v[64:67]\n"
    "s_waitcnt vmcnt(9)\n"
    "s_barrier\n"
    "s_setprio 3\n"
    "ds_read_b64_tr_b16 v[48:49], v40 offset:32768\n"
    "ds_read_b64_tr_b16 v[50:51], v40 offset:33792\n"
    "v_mfma_f32_16x16x32_bf16 v[68:71], v[52:55], v[140:143], v[68:71]\n"
    "buffer_store_dwordx4 v[8:11], v3, %[rw], s50 offen sc1\n"
    "s_waitcnt lgkmcnt(7)\n"
    "v_mfma_f32_16x16x32_bf16 v[72:75], v[56:59], v[136:139], v[72:75]\n"
    "ds_read_b64_tr_b16 v[52:53], v41 offset:32768\n"
    "ds_read_b64_tr_b16 v[54:55], v41 offset:33792\n"
    "v_mfma_f32_16x16x32_bf16 v[76:79], v[56:59], v[140:143], v[76:79]\n"
    "s_waitcnt lgkmcnt(7)\n"
    "v_mfma_f32_16x16x32_bf16 v[80:83], v[60:63], v[136:139], v[80:83]\n"
    "s_setprio 2\n"
    "ds_read_b64_tr_b16 v[56:57], v42 offset:32768\n"
    "ds_read_b64_tr_b16 v[58:59], v42 offset:33792\n"
    "v_mfma_f32_16x16x32_bf16 v[84:87], v[60:63], v[140:143], v[84:87]\n"
    "s_waitcnt lgkmcnt(6)\n"
    "v_mfma_f32_16x16x32_bf16 v[88:91], v[44:47], v[136:139], v[88:91]\n"
    "ds_read_b64_tr_b16 v[60:61], v43 offset:32768\n"
    "ds_read_b64_tr_b16 v[62:63], v43 offset:33792\n"
    "v_mfma_f32_16x16x32_bf16 v[92:95], v[44:47], v[140:143], v[92:95]\n"
    "s_waitcnt lgkmcnt(6)\n"
    "v_mfma_f32_16x16x32_bf16 v[32:35], v[36:39], v[48:51], v[32:35]\n"
    "v_mfma_f32_16x16x32_bf16 v[64:67], v[48:51], v[144:147], v[64:67]\n"
    "s_setprio 1\n"
    "ds_read_b64_tr_b16 v[44:45], v40 offset:40960\n"
    "ds_read_b64_tr_b16 v[46:47], v40 offset:41984\n"
    "v_mfma_f32_16x16x32_bf16 v[68:71], v[48:51], v[148:151], v[68:71]\n"
    "s_add_u32 s56, s56, 1\n"
    "s_cmp_eq_u32 s56, 10\n"
    "s_cselect_b32 s56, 0, s56\n"
    "s_add_u32 m0, s40, 98304\n"
    "s_mul_i32 s57, s56, s55\n"
    "buffer_load_dwordx4 v23, %[r1], s57 offen  lds\n"
    "s_waitcnt lgkmcnt(6)\n"
    "v_mfma_f32_16x16x32_bf16 v[72:75], v[52:55], v[144:147], v[72:75]\n"
    "ds_read_b64_tr_b16 v[48:49], v41 offset:40960\n"
    "ds_read_b64_tr_b16 v[50:51], v41 offset:41984\n"
    "v_mfma_f32_16x16x32_bf16 v[76:79], v[52:55], v[148:151], v[76:79]\n"
    "s_add_u32 m0, s40, 99328\n"
    "s_add_u32 s57, s57, s59\n"
    "buffer_load_dwordx4 v23, %[r1], s57 offen  lds\n"
    "s_waitcnt lgkmcnt(6)\n"
    "v_mfma_f32_16x16x32_bf16 v[80:83], v[56:59], v[144:147], v[80:83]\n"
    "s_setprio 0\n"
    "ds_read_b64_tr_b16 v[52:53], v42 offset:40960\n"
    "ds_read_b64_tr_b16 v[54:55], v42 offset:41984\n"
    "v_mfma_f32_16x16x32_bf16 v[84:87], v[56:59], v[148:151], v[84:87]\n"
    "s_waitcnt lgkmcnt(6)\n"
    "v_mfma_f32_16x16x32_bf16 v[88:91], v[60:63], v[144:147], v[88:91]\n"
    "ds_read_b64_tr_b16 v[56:57], v43 offset:40960\n"
    "ds_read_b64_tr_b16 v[58:59], v43 offset:41984\n"
    "v_mfma_f32_16x16x32_bf16 v[92:95], v[60:63], v[148:151], v[92:95]\n"
    "s_add_u32 s56, s56, 1\n"
    "s_cmp_eq_u32 s56, 10\n"
    "s_cselect_b32 s56, 0, s56\n"
    "s_add_u32 m0, s40, 114688\n"
    "s_mul_i32 s57, s56, s55\n"
    "buffer_load_dwordx4 v23, %[r1], s57 offen  lds\n"
    "s_waitcnt lgkmcnt(6)\n"
    "v_mfma_f32_16x16x32_bf16 v[32:35], v[36:39], v[44:47], v[32:35]\n"
    "v_mfma_f32_16x16x32_bf16 v[64:67], v[44:47], v[152:155], v[64:67]\n"
    "s_waitcnt vmcnt(11)\n"
    "s_barrier\n"
    "s_setprio 3\n"
    "ds_read_b64_tr_b16 v[60:61], v40 offset:49152\n"
    "ds_read_b64_tr_b16 v[62:63], v40 offset:50176\n"
    "v_mfma_f32_16x16x32_bf16 v[68:71], v[44:47], v[156:159], v[68:71]\n"
    "s_waitcnt lgkmcnt(6)\n"
    "v_mfma_f32_16x16x32_bf16 v[72:75], v[48:51], v[152:155], v[72:75]\n"
    "ds_read_b64_tr_b16 v[44:45], v41 offset:49152\n"
    "ds_read_b64_tr_b16 v[46:47], v41 offset:50176\n"
    "v_mfma_f32_16x16x32_bf16 v[76:79], v[48:51], v[156:159], v[76:79]\n"
    "s_add_u32 m0, s40, 115712\n"
    "s_add_u32 s57, s57, s59\n"
    "buffer_load_dwordx4 v23, %[r1], s57 offen  lds\n"
    "s_waitcnt lgkmcnt(6)\n"
    "v_mfma_f32_16x16x32_bf16 v[80:83], v[52:55], v[152:155], v[80:83]\n"
    "s_setprio 2\n"
    "ds_read_b64_tr_b16 v[48:49], v42 offset:49152\n"
    "ds_read_b64_tr_b16 v[50:51], v42 offset:50176\n"
    "v_mfma_f32_16x16x32_bf16 v[84:87], v[52:55], v[156:159], v[84:87]\n"
    "s_waitcnt lgkmcnt(6)\n"
    "v_mfma_f32_16x16x32_bf16 v[88:91], v[56:59], v[152:155], v[88:91]\n"
    "ds_read_b64_tr_b16 v[52:53], v43 offset:49152\n"
    "ds_read_b64_tr_b16 v[54:55], v43 offset:50176\n"
    "v_mfma_f32_16x16x32_bf16 v[92:95], v[56:59], v[156:159], v[92:95]\n"
    "s_add_u32 s56, s56, 1\n"
    "s_cmp_eq_u32 s56, 10\n"
    "s_cselect_b32 s56, 0, s56\n"
    "s_add_u32 m0, s40, 131072\n"
    "s_mul_i32 s57, s56, s55\n"
    "buffer_load_dwordx4 v23, %[r1], s57 offen  lds\n"
    "s_waitcnt lgkmcnt(6)\n"
    "v_mfma_f32_16x16x32_bf16 v[32:35], v[36:39], v[60:63], v[32:35]\n"
    "v_mfma_f32_16x16x32_bf16 v[64:67], v[60:63], v[160:163], v[64:67]\n"
    "s_setprio 1\n"
    "ds_read_b64_tr_b16 v[56:57], v40 offset:57344\n"
    "ds_read_b64_tr_b16 v[58:59], v40 offset:58368\n"
    "v_mfma_f32_16x16x32_bf16 v[68:71], v[60:63], v[164:167], v[68:71]\n"
    "s_waitcnt lgkmcnt(6)\n"
    "v_mfma_f32_16x16x32_bf16 v[72:75], v[44:47], v[160:163], v[72:75]\n"
    "ds_read_b64_tr_b16 v[60:61], v41 offset:57344\n"
    "ds_read_b64_tr_b16 v[62:63], v41 offset:58368\n"
    "v_mfma_f32_16x16x32_bf16 v[76:79], v[44:47], v[164:167], v[76:79]\n"
    "s_add_u32 m0, s40, 132096\n"
    "s_add_u32 s57, s57, s59\n"
    "buffer_load_dwordx4 v23, %[r1], s57 offen  lds\n"
    "s_waitcnt lgkmcnt(6)\n"
    "v_mfma_f32_16x16x32_bf16 v[80:83], v[48:51], v[160:163], v[80:83]\n"
    "s_setprio 0\n"
    "ds_read_b64_tr_b16 v[44:45], v42 offset:57344\n"
    "ds_read_b64_tr_b16 v[46:47], v42 offset:58368\n"
    "v_mfma_f32_16x16x32_bf16 v[84:87], v[48:51], v[164:167], v[84:87]\n"
    "s_waitcnt lgkmcnt(6)\n"
    "v_mfma_f32_16x16x32_bf16 v[88:91], v[52:55], v[160:163], v[88:91]\n"
    "ds_read_b64_tr_b16 v[48:49], v43 offset:57344\n"
    "ds_read_b64_tr_b16 v[50:51], v43 offset:58368\n"
    "v_mfma_f32_16x16x32_bf16 v[92:95], v[52:55], v[164:167], v[92:95]\n"
    "s_waitcnt lgkmcnt(6)\n"
    "v_mfma_f32_16x16x32_bf16 v[32:35], v[36:39], v[56:59], v[32:35]\n"
    "v_mfma_f32_16x16x32_bf16 v[64:67], v[56:59], v[168:171], v[64:67]\n"
    "s_waitcnt vmcnt(12)\n"
    "s_barrier\n"
    "s_setprio 3\n"
    "v_add_u32 v40, 0x10000, v40\n"
    "v_add_u32 v41, 0x10000, v41\n"
    "v_add_u32 v42, 0x10000, v42\n"
    "v_add_u32 v43, 0x10000, v43\n"
    "ds_read_b64_tr_b16 v[52:53], v40 offset:0\n"
    "ds_read_b64_tr_b16 v[54:55], v40 offset:1024\n"
    "v_mfma_f32_16x16x32_bf16 v[68:71], v[56:59], v[172:175], v[68:71]\n"
    "s_waitcnt lgkmcnt(6)\n"
    "v_mfma_f32_16x16x32_bf16 v[72:75], v[60:63], v[168:171], v[72:75]\n"
    "ds_read_b64_tr_b16 v[56:57], v41 offset:0\n"
    "ds_read_b64_tr_b16 v[58:59], v41 offset:1024\n"
    "v_mfma_f32_16x16x32_bf16 v[76:79], v[60:63], v[172:175], v[76:79]\n"
    "s_waitcnt lgkmcnt(6)\n"
    "v_mfma_f32_16x16x32_bf16 v[80:83], v[44:47], v[168:171], v[80:83]\n"
    "s_setprio 2\n"
    "ds_read_b64_tr_b16 v[60:61], v42 offset:0\n"
    "ds_read_b64_tr_b16 v[62:63], v42 offset:1024\n"
    "v_mfma_f32_16x16x32_bf16 v[84:87], v[44:47], v[172:175], v[84:87]\n"
    "s_waitcnt lgkmcnt(6)\n"
    "v_mfma_f32_16x16x32_bf16 v[88:91], v[48:51], v[168:171], v[88:91]\n"
    "ds_read_b64_tr_b16 v[44:45], v43 offset:0\n"
    "ds_read_b64_tr_b16 v[46:47], v43 offset:1024\n"
    "v_mfma_f32_16x16x32_bf16 v[92:95], v[48:51], v[172:175], v[92:95]\n"
    "s_waitcnt lgkmcnt(6)\n"
    "v_mfma_f32_16x16x32_bf16 v[32:35], v[36:39], v[52:55], v[32:35]\n"
    "v_mfma_f32_16x16x32_bf16 v[64:67], v[52:55], v[176:179], v[64:67]\n"
    "s_setprio 1\n"
    "ds_read_b64_tr_b16 v[48:49], v40 offset:8192\n"
    "ds_read_b64_tr_b16 v[50:51], v40 offset:9216\n"
    "v_mfma_f32_16x16x32_bf16 v[68:71], v[52:55], v[180:183], v[68:71]\n"
    "s_waitcnt lgkmcnt(6)\n"
    "v_mfma_f32_16x16x32_bf16 v[72:75], v[56:59], v[176:179], v[72:75]\n"
    "ds_read_b64_tr_b16 v[52:53], v41 offset:8192\n"
    "ds_read_b64_tr_b16 v[54:55], v41 offset:9216\n"
    "v_mfma_f32_16x16x32_bf16 v[76:79], v[56:59], v[180:183], v[76:79]\n"
    "s_waitcnt lgkmcnt(6)\n"
    "v_mfma_f32_16x16x32_bf16 v[80:83], v[60:63], v[176:179], v[80:83]\n"
    "s_setprio 0\n"
    "ds_read_b64_tr_b16 v[56:57], v42 offset:8192\n"
    "ds_read_b64_tr_b16 v[58:59], v42 offset:9216\n"
    "v_mfma_f32_16x16x32_bf16 v[84:87], v[60:63], v[180:183], v[84:87]\n"
    "s_waitcnt lgkmcnt(6)\n"
    "v_mfma_f32_16x16x32_bf16 v[88:91], v[44:47], v[176:179], v[88:91]\n"
    "ds_read_b64_tr_b16 v[60:61], v43 offset:8192\n"
    "ds_read_b64_tr_b16 v[62:63], v43 offset:9216\n"
    "v_mfma_f32_16x16x32_bf16 v[92:95], v[44:47], v[180:183], v[92:95]\n"
    "s_waitcnt lgkmcnt(6)\n"
    "v_mfma_f32_16x16x32_bf16 v[32:35], v[36:39], v[48:51], v[32:35]\n"
    "v_mfma_f32_16x16x32_bf16 v[64:67], v[48:51], v[184:187], v[64:67]\n"
    "s_waitcnt vmcnt(0)\n"
    "s_barrier\n"
    "v_mov_b32 v96, s60\n"
    "v_mov_b32 v97, s61\n"
    "s_cmp_lg_u32 s40, 0\n"
    "s_cbranch_scc1 2f\n"
    "s_mov_b64 exec, 1\n"
    "buffer_store_dwordx2 v[96:97], off, %[rw], s51 sc1\n"
    "s_mov_b64 exec, -1\n"
    "2:\n"
    "s_setprio 3\n"
    "ds_read_b64_tr_b16 v[44:45], v40 offset:16384\n"
    "ds_read_b64_tr_b16 v[46:47], v40 offset:17408\n"
    "v_mfma_f32_16x16x32_bf16 v[68:71], v[48:51], v[188:191], v[68:71]\n"
    "v_xor_b32 v104, 0, v7\n"
    "buffer_load_ushort v105, v104, %[rb], 0 offen\n"
    "s_cmp_ge_u32 s40, 8192\n"
    "s_cbranch_scc1 .Lp1_m\n"
    "s_setprio 1\n"
    "s_waitcnt lgkmcnt(6)\n"
    "v_mfma_f32_16x16x32_bf16 v[72:75], v[52:55], v[184:187], v[72:75]\n"
    "ds_read_b64_tr_b16 v[48:49], v41 offset:16384\n"
    "ds_read_b64_tr_b16 v[50:51], v41 offset:17408\n"
    "s_add_u32 s50, s47, 2686976\n"
    "s_lshl_b32 s64, s46, 12\n"
    "v_mfma_f32_16x16x32_bf16 v[76:79], v[52:55], v[188:191], v[76:79]\n"
    "s_lshr_b32 s65, s40, 1\n"
    "s_add_u32 s64, s64, s65\n"
    "v_xor_b32 v106, 32, v7\n"
    "buffer_load_ushort v107, v106, %[rb], 0 offen\n"
    "s_waitcnt lgkmcnt(6)\n"
    "v_mfma_f32_16x16x32_bf16 v[80:83], v[56:59], v[184:187], v[80:83]\n"
    "ds_read_b64_tr_b16 v[52:53], v42 offset:16384\n"
    "ds_read_b64_tr_b16 v[54:55], v42 offset:17408\n"
    "s_add_u32 s64, s64, 65536\n"
    "s_add_u32 s49, s64, s47\n"
    "v_mfma_f32_16x16x32_bf16 v[84:87], v[56:59], v[188:191], v[84:87]\n"
    "s_add_u32 s52, s64, s48\n"
    "s_add_u32 s55, s48, 2686976\n"
    "v_xor_b32 v108, 64, v7\n"
    "buffer_load_ushort v109, v108, %[rb], 0 offen\n"
    "s_waitcnt lgkmcnt(6)\n"
    "v_mfma_f32_16x16x32_bf16 v[88:91], v[60:63], v[184:187], v[88:91]\n"
    "ds_read_b64_tr_b16 v[56:57], v43 offset:16384\n"
    "ds_read_b64_tr_b16 v[58:59], v43 offset:17408\n"
    "v_mfma_f32_16x16x32_bf16 v[92:95], v[60:63], v[188:191], v[92:95]\n"
    "v_xor_b32 v110, 96, v7\n"
    "buffer_load_ushort v111, v110, %[rb], 0 offen\n"
    "s_waitcnt lgkmcnt(6)\n"
    "v_mfma_f32_16x16x32_bf16 v[32:35], v[36:39], v[44:47], v[32:35]\n"
    "v_mfma_f32_16x16x32_bf16 v[64:67], v[44:47], v[192:195], v[64:67]\n"
    "ds_read_b64_tr_b16 v[60:61], v40 offset:24576\n"
    "ds_read_b64_tr_b16 v[62:63], v40 offset:25600\n"
    "v_mfma_f32_16x16x32_bf16 v[68:71], v[44:47], v[196:199], v[68:71]\n"
    "s_waitcnt lgkmcnt(6)\n"
    "v_mfma_f32_16x16x32_bf16 v[72:75], v[48:51], v[192:195], v[72:75]\n"
    "ds_read_b64_tr_b16 v[44:45], v41 offset:24576\n"
    "ds_read_b64_tr_b16 v[46:47], v41 offset:25600\n"
    "v_mfma_f32_16x16x32_bf16 v[76:79], v[48:51], v[196:199], v[76:79]\n"
    "s_waitcnt lgkmcnt(6)\n"
    "v_mfma_f32_16x16x32_bf16 v[80:83], v[52:55], v[192:195], v[80:83]\n"
    "ds_read_b64_tr_b16 v[48:49], v42 offset:24576\n"
    "ds_read_b64_tr_b16 v[50:51], v42 offset:25600\n"
    "v_mfma_f32_16x16x32_bf16 v[84:87], v[52:55], v[196:199], v[84:87]\n"
    "s_waitcnt lgkmcnt(6)\n"
    "v_mfma_f32_16x16x32_bf16 v[88:91], v[56:59], v[192:195], v[88:91]\n"
    "ds_read_b64_tr_b16 v[52:53], v43 offset:24576\n"
    "ds_read_b64_tr_b16 v[54:55], v43 offset:25600\n"
    "v_mfma_f32_16x16x32_bf16 v[92:95], v[56:59], v[196:199], v[92:95]\n"
    "s_waitcnt lgkmcnt(6)\n"
    "v_mfma_f32_16x16x32_bf16 v[32:35], v[36:39], v[60:63], v[32:35]\n"
    "v_mfma_f32_16x16x32_bf16 v[64:67], v[60:63], v[200:203], v[64:67]\n"
    "ds_read_b64_tr_b16 v[56:57], v40 offset:32768\n"
    "ds_read_b64_tr_b16 v[58:59], v40 offset:33792\n"
    "v_mfma_f32_16x16x32_bf16 v[68:71], v[60:63], v[204:207], v[68:71]\n"
    "s_waitcnt lgkmcnt(6)\n"
    "v_mfma_f32_16x16x32_bf16 v[72:75], v[44:47], v[200:203], v[72:75]\n"
    "ds_read_b64_tr_b16 v[60:61], v41 offset:32768\n"
    "ds_read_b64_tr_b16 v[62:63], v41 offset:33792\n"
    "v_mfma_f32_16x16x32_bf16 v[76:79], v[44:47], v[204:207], v[76:79]\n"
    "s_waitcnt lgkmcnt(6)\n"
    "v_mfma_f32_16x16x32_bf16 v[80:83], v[48:51], v[200:203], v[80:83]\n"
    "ds_read_b64_tr_b16 v[44:45], v42 offset:32768\n"
    "ds_read_b64_tr_b16 v[46:47], v42 offset:33792\n"
    "v_mfma_f32_16x16x32_bf16 v[84:87], v[48:51], v[204:207], v[84:87]\n"
    "s_waitcnt lgkmcnt(6)\n"
    "v_mfma_f32_16x16x32_bf16 v[88:91], v[52:55], v[200:203], v[88:91]\n"
    "ds_read_b64_tr_b16 v[48:49], v43 offset:32768\n"
    "ds_read_b64_tr_b16 v[50:51], v43 offset:33792\n"
    "v_mfma_f32_16x16x32_bf16 v[92:95], v[52:55], v[204:207], v[92:95]\n"
    "s_waitcnt lgkmcnt(6)\n"
    "v_mfma_f32_16x16x32_bf16 v[32:35], v[36:39], v[56:59], v[32:35]\n"
    "v_mfma_f32_16x16x32_bf16 v[64:67], v[56:59], v[208:211], v[64:67]\n"
    "ds_read_b64_tr_b16 v[52:53], v40 offset:40960\n"
    "ds_read_b64_tr_b16 v[54:55], v40 offset:41984\n"
    "v_mfma_f32_16x16x32_bf16 v[68:71], v[56:59], v[212:215], v[68:71]\n"
    "s_waitcnt lgkmcnt(6)\n"
    "v_mfma_f32_16x16x32_bf16 v[72:75], v[60:63], v[208:211], v[72:75]\n"
    "ds_read_b64_tr_b16 v[56:57], v41 offset:40960\n"
    "ds_read_b64_tr_b16 v[58:59], v41 offset:41984\n"
    "v_mfma_f32_16x16x32_bf16 v[76:79], v[60:63], v[212:215], v[76:79]\n"
    "s_waitcnt lgkmcnt(6)\n"
    "v_mfma_f32_16x16x32_bf16 v[80:83], v[44:47], v[208:211], v[80:83]\n"
    "ds_read_b64_tr_b16 v[60:61], v42 offset:40960\n"
    "ds_read_b64_tr_b16 v[62:63], v42 offset:41984\n"
    "v_mfma_f32_16x16x32_bf16 v[84:87], v[44:47], v[212:215], v[84:87]\n"
    "s_waitcnt lgkmcnt(6)\n"
    "v_mfma_f32_16x16x32_bf16 v[88:91], v[48:51], v[208:211], v[88:91]\n"
    "ds_read_b64_tr_b16 v[44:45], v43 offset:40960\n"
    "ds_read_b64_tr_b16 v[46:47], v43 offset:41984\n"
    "v_mfma_f32_16x16x32_bf16 v[92:95], v[48:51], v[212:215], v[92:95]\n"
    "s_waitcnt lgkmcnt(6)\n"
    "v_mfma_f32_16x16x32_bf16 v[32:35], v[36:39], v[52:55], v[32:35]\n"
    "v_mfma_f32_16x16x32_bf16 v[64:67], v[52:55], v[216:219], v[64:67]\n"
    "ds_read_b64_tr_b16 v[48:49], v40 offset:49152\n"
    "ds_read_b64_tr_b16 v[50:51], v40 offset:50176\n"
    "v_mfma_f32_16x16x32_bf16 v[68:71], v[52:55], v[220:223], v[68:71]\n"
    "s_waitcnt lgkmcnt(6)\n"
    "v_mfma_f32_16x16x32_bf16 v[72:75], v[56:59], v[216:219], v[72:75]\n"
    "ds_read_b64_tr_b16 v[52:53], v41 offset:49152\n"
    "ds_read_b64_tr_b16 v[54:55], v41 offset:50176\n"
    "v_mfma_f32_16x16x32_bf16 v[76:79], v[56:59], v[220:223], v[76:79]\n"
    "s_waitcnt lgkmcnt(6)\n"
    "v_mfma_f32_16x16x32_bf16 v[80:83], v[60:63], v[216:219], v[80:83]\n"
    "ds_read_b64_tr_b16 v[56:57], v42 offset:49152\n"
    "ds_read_b64_tr_b16 v[58:59], v42 offset:50176\n"
    "v_mfma_f32_16x16x32_bf16 v[84:87], v[60:63], v[220:223], v[84:87]\n"
    "s_cmp_lg_u32 s40, 0\n"
    "s_cbranch_scc1 2f\n"
    "buffer_load_dwordx2 v[112:113], v12, %[rw], s53 offen sc1\n"
    "buffer_load_dwordx2 v[114:115], v13, %[rw], s54 offen sc1\n"
    "2:\n"
    "s_waitcnt lgkmcnt(6)\n"
    "v_mfma_f32_16x16x32_bf16 v[88:91], v[44:47], v[216:219], v[88:91]\n"
    "ds_read_b64_tr_b16 v[60:61], v43 offset:49152\n"
    "ds_read_b64_tr_b16 v[62:63], v43 offset:50176\n"
    "v_mfma_f32_16x16x32_bf16 v[92:95], v[44:47], v[220:223], v[92:95]\n"
    "s_waitcnt lgkmcnt(6)\n"
    "v_mfma_f32_16x16x32_bf16 v[32:35], v[36:39], v[48:51], v[32:35]\n"
    "v_mfma_f32_16x16x32_bf16 v[64:67], v[48:51], v[224:227], v[64:67]\n"
    "ds_read_b64_tr_b16 v[44:45], v40 offset:57344\n"
    "ds_read_b64_tr_b16 v[46:47], v40 offset:58368\n"
    "v_mfma_f32_16x16x32_bf16 v[68:71], v[48:51], v[228:231], v[68:71]\n"
    "s_waitcnt lgkmcnt(6)\n"
    "v_mfma_f32_16x16x32_bf16 v[72:75], v[52:55], v[224:227], v[72:75]\n"
    "ds_read_b64_tr_b16 v[48:49], v41 offset:57344\n"
    "ds_read_b64_tr_b16 v[50:51], v41 offset:58368\n"
    "v_mfma_f32_16x16x32_bf16 v[76:79], v[52:55], v[228:231], v[76:79]\n"
    "s_waitcnt lgkmcnt(6)\n"
    "v_mfma_f32_16x16x32_bf16 v[80:83], v[56:59], v[224:227], v[80:83]\n"
    "ds_read_b64_tr_b16 v[52:53], v42 offset:57344\n"
    "ds_read_b64_tr_b16 v[54:55], v42 offset:58368\n"
    "v_mfma_f32_16x16x32_bf16 v[84:87], v[56:59], v[228:231], v[84:87]\n"
    "s_waitcnt lgkmcnt(6)\n"
    "v_mfma_f32_16x16x32_bf16 v[88:91], v[60:63], v[224:227], v[88:91]\n"
    "ds_read_b64_tr_b16 v[56:57], v43 offset:57344\n"
    "ds_read_b64_tr_b16 v[58:59], v43 offset:58368\n"
    "v_mfma_f32_16x16x32_bf16 v[92:95], v[60:63], v[228:231], v[92:95]\n"
    "s_waitcnt lgkmcnt(6)\n"
    "v_mfma_f32_16x16x32_bf16 v[32:35], v[36:39], v[44:47], v[32:35]\n"
    "v_mfma_f32_16x16x32_bf16 v[64:67], v[44:47], v[232:235], v[64:67]\n"
    "v_add_u32 v40, 0x10000, v40\n"
    "v_add_u32 v41, 0x10000, v41\n"
    "v_add_u32 v42, 0x10000, v42\n"
    "v_add_u32 v43, 0x10000, v43\n"
    "ds_read_b64_tr_b16 v[60:61], v40 offset:0\n"
    "ds_read_b64_tr_b16 v[62:63], v40 offset:1024\n"
    "v_mfma_f32_16x16x32_bf16 v[68:71], v[44:47], v[236:239], v[68:71]\n"
    "s_waitcnt lgkmcnt(6)\n"
    "v_mfma_f32_16x16x32_bf16 v[72:75], v[48:51], v[232:235], v[72:75]\n"
    "ds_read_b64_tr_b16 v[44:45], v40 offset:8192\n"
    "ds_read_b64_tr_b16 v[46:47], v40 offset:9216\n"
    "v_mfma_f32_16x16x32_bf16 v[76:79], v[48:51], v[236:239], v[76:79]\n"
    "s_waitcnt lgkmcnt(6)\n"
    "v_mfma_f32_16x16x32_bf16 v[80:83], v[52:55], v[232:235], v[80:83]\n"
    "ds_read_b64_tr_b16 v[48:49], v41 offset:0\n"
    "ds_read_b64_tr_b16 v[50:51], v41 offset:1024\n"
    "v_mfma_f32_16x16x32_bf16 v[84:87], v[52:55], v[236:239], v[84:87]\n"
    "s_waitcnt lgkmcnt(6)\n"
    "v_mfma_f32_16x16x32_bf16 v[88:91], v[56:59], v[232:235], v[88:91]\n"
    "ds_read_b64_tr_b16 v[52:53], v42 offset:0\n"
    "ds_read_b64_tr_b16 v[54:55], v42 offset:1024\n"
    "v_mfma_f32_16x16x32_bf16 v[92:95], v[56:59], v[236:239], v[92:95]\n"
    "s_waitcnt lgkmcnt(6)\n"
    "v_mfma_f32_16x16x32_bf16 v[32:35], v[36:39], v[60:63], v[32:35]\n"
    "v_mfma_f32_16x16x32_bf16 v[64:67], v[60:63], v[240:243], v[64:67]\n"
    "ds_read_b64_tr_b16 v[56:57], v43 offset:0\n"
    "ds_read_b64_tr_b16 v[58:59], v43 offset:1024\n"
    "v_mfma_f32_16x16x32_bf16 v[68:71], v[60:63], v[244:247], v[68:71]\n"
    "s_waitcnt lgkmcnt(6)\n"
    "v_mfma_f32_16x16x32_bf16 v[32:35], v[36:39], v[44:47], v[32:35]\n"
    "v_mfma_f32_16x16x32_bf16 v[64:67], v[44:47], v[248:251], v[64:67]\n"
    "ds_read_b64_tr_b16 v[60:61], v41 offset:8192\n"
    "ds_read_b64_tr_b16 v[62:63], v41 offset:9216\n"
    "v_mfma_f32_16x16x32_bf16 v[68:71], v[44:47], v[252:255], v[68:71]\n"
    "s_waitcnt lgkmcnt(6)\n"
    "v_mfma_f32_16x16x32_bf16 v[72:75], v[48:51], v[240:243], v[72:75]\n"
    "ds_read_b64_tr_b16 v[44:45], v42 offset:8192\n"
    "ds_read_b64_tr_b16 v[46:47], v42 offset:9216\n"
    "v_mfma_f32_16x16x32_bf16 v[76:79], v[48:51], v[244:247], v[76:79]\n"
    "s_nop 1\n"
    "v_cvt_pk_bf16_f32 v33, v32, v32\n"
    "v_lshlrev_b32 v33, 16, v33\n"
    "v_sub_f32 v33, v32, v33\n"
    "v_cvt_pk_bf16_f32 v32, v32, v33\n"
    "ds_write_b32 v21, v32\n"
    "s_waitcnt lgkmcnt(7)\n"
    "v_mfma_f32_16x16x32_bf16 v[80:83], v[52:55], v[240:243], v[80:83]\n"
    "ds_read_b64_tr_b16 v[48:49], v43 offset:8192\n"
    "ds_read_b64_tr_b16 v[50:51], v43 offset:9216\n"
    "v_mfma_f32_16x16x32_bf16 v[84:87], v[52:55], v[244:247], v[84:87]\n"
    "s_waitcnt lgkmcnt(7)\n"
    "v_mfma_f32_16x16x32_bf16 v[88:91], v[56:59], v[240:243], v[88:91]\n"
    "v_mfma_f32_16x16x32_bf16 v[92:95], v[56:59], v[244:247], v[92:95]\n"
    "s_cmp_lg_u32 s40, 0\n"
    "s_cbranch_scc1 4f\n"
    "s_waitcnt vmcnt(0)\n"
    "s_mov_b32 s58, 1048576\n"
    "3:\n"
    "v_cmp_ne_u32 s[62:63], s60, v112\n"
    "s_mov_b64 vcc, s[62:63]\n"
    "v_cmp_ne_u32 s[62:63], s61, v113\n"
    "s_or_b64 vcc, vcc, s[62:63]\n"
    "v_cmp_ne_u32 s[62:63], s60, v114\n"
    "s_or_b64 vcc, vcc, s[62:63]\n"
    "v_cmp_ne_u32 s[62:63], s61, v115\n"
    "s_or_b64 vcc, vcc, s[62:63]\n"
    "s_cbranch_scc0 4f\n"
    "s_sub_u32 s58, s58, 1\n"
    "s_cbranch_scc1 4f\n"
    "s_sleep 2\n"
    "buffer_load_dwordx2 v[112:113], v12, %[rw], s53 offen sc1\n"
    "buffer_load_dwordx2 v[114:115], v13, %[rw], s54 offen sc1\n"
    "s_waitcnt vmcnt(0)\n"
    "s_branch 3b\n"
    "4:\n"
    "s_waitcnt vmcnt(0) lgkmcnt(0)\n"
    "s_barrier\n"
    "s_setprio 3\n"
    "buffer_load_dwordx2 v[216:217], v6, %[rw], s55 offen offset:0 sc1\n"
    "buffer_load_dwordx2 v[200:201], v6, %[rw], s52 offen offset:0 sc1\n"
    "buffer_load_dwordx4 v[204:207], v5, %[rw], s50 offen offset:0 sc1\n"
    "buffer_load_dwordx4 v[192:195], v5, %[rw], s49 offen offset:0 sc1\n"
    "buffer_load_dwordx4 v[196:199], v5, %[rw], s49 offen offset:512 sc1\n"
    "v_xor_b32 v32, 0, v21\n"
    "v_xor_b32 v33, 64, v21\n"
    "v_xor_b32 v34, 128, v21\n"
    "v_xor_b32 v35, 192, v21\n"
    "ds_read_b32 v104, v32\n"
    "ds_read_b32 v106, v33\n"
    "ds_read_b32 v108, v34\n"
    "ds_read_b32 v110, v35\n"
    "v_mfma_f32_16x16x32_bf16 v[72:75], v[60:63], v[248:251], v[72:75]\n"
    "v_mfma_f32_16x16x32_bf16 v[76:79], v[60:63], v[252:255], v[76:79]\n"
    "v_mfma_f32_16x16x32_bf16 v[80:83], v[44:47], v[248:251], v[80:83]\n"
    "v_mfma_f32_16x16x32_bf16 v[84:87], v[44:47], v[252:255], v[84:87]\n"
    "v_mfma_f32_16x16x32_bf16 v[88:91], v[48:51], v[248:251], v[88:91]\n"
    "v_mfma_f32_16x16x32_bf16 v[92:95], v[48:51], v[252:255], v[92:95]\n"
    "s_waitcnt lgkmcnt(0)\n"
    "v_mfma_f32_16x16x16_bf16 v[64:67], v[104:105], v[28:29], v[64:67]\n"
    "v_mfma_f32_16x16x16_bf16 v[72:75], v[106:107], v[28:29], v[72:75]\n"
    "v_mfma_f32_16x16x16_bf16 v[68:71], v[104:105], v[24:25], v[68:71]\n"
    "v_mfma_f32_16x16x16_bf16 v[76:79], v[106:107], v[24:25], v[76:79]\n"
    "v_mfma_f32_16x16x16_bf16 v[80:83], v[108:109], v[28:29], v[80:83]\n"
    "v_mfma_f32_16x16x16_bf16 v[88:91], v[110:111], v[28:29], v[88:91]\n"
    "v_mfma_f32_16x16x16_bf16 v[84:87], v[108:109], v[24:25], v[84:87]\n"
    "v_mfma_f32_16x16x16_bf16 v[92:95], v[110:111], v[24:25], v[92:95]\n"
    "s_branch .Lp1_e\n"
    ".Lp1_x:\n"
    "buffer_store_dwordx4 v[16:19], v2, %[rw], s50 offen sc1\n"
    "buffer_store_dwordx4 v[8:11], v3, %[rw], s50 offen sc1\n"
    "v_mov_b32 v96, s60\n"
    "v_mov_b32 v97, s61\n"
    "s_waitcnt lgkmcnt(0)\n"
    "s_setprio 0\n"
    "s_branch .Lp1_e\n"
    ".Lp1_m:\n"
    "s_setprio 3\n"
    "s_waitcnt lgkmcnt(6)\n"
    "v_mfma_f32_16x16x32_bf16 v[72:75], v[52:55], v[184:187], v[72:75]\n"
    "ds_read_b64_tr_b16 v[48:49], v41 offset:16384\n"
    "ds_read_b64_tr_b16 v[50:51], v41 offset:17408\n"
    "v_mfma_f32_16x16x32_bf16 v[76:79], v[52:55], v[188:191], v[76:79]\n"
    "v_xor_b32 v106, 32, v7\n"
    "buffer_load_ushort v107, v106, %[rb], 0 offen\n"
    "s_waitcnt lgkmcnt(6)\n"
    "v_mfma_f32_16x16x32_bf16 v[80:83], v[56:59], v[184:187], v[80:83]\n"
    "ds_read_b64_tr_b16 v[52:53], v42 offset:16384\n"
    "ds_read_b64_tr_b16 v[54:55], v42 offset:17408\n"
    "v_mfma_f32_16x16x32_bf16 v[84:87], v[56:59], v[188:191], v[84:87]\n"
    "v_xor_b32 v108, 64, v7\n"
    "buffer_load_ushort v109, v108, %[rb], 0 offen\n"
    "s_waitcnt lgkmcnt(6)\n"
    "v_mfma_f32_16x16x32_bf16 v[88:91], v[60:63], v[184:187], v[88:91]\n"
    "ds_read_b64_tr_b16 v[56:57], v43 offset:16384\n"
    "ds_read_b64_tr_b16 v[58:59], v43 offset:17408\n"
    "v_mfma_f32_16x16x32_bf16 v[92:95], v[60:63], v[188:191], v[92:95]\n"
    "v_xor_b32 v110, 96, v7\n"
    "buffer_load_ushort v111, v110, %[rb], 0 offen\n"
    "s_waitcnt lgkmcnt(6)\n"
    "v_mfma_f32_16x16x32_bf16 v[32:35], v[36:39], v[44:47], v[32:35]\n"
    "v_mfma_f32_16x16x32_bf16 v[64:67], v[44:47], v[192:195], v[64:67]\n"
    "ds_read_b64_tr_b16 v[60:61], v40 offset:24576\n"
    "ds_read_b64_tr_b16 v[62:63], v40 offset:25600\n"
    "v_mfma_f32_16x16x32_bf16 v[68:71], v[44:47], v[196:199], v[68:71]\n"
    "s_waitcnt lgkmcnt(6)\n"
    "v_mfma_f32_16x16x32_bf16 v[72:75], v[48:51], v[192:195], v[72:75]\n"
    "ds_read_b64_tr_b16 v[44:45], v41 offset:24576\n"
    "ds_read_b64_tr_b16 v[46:47], v41 offset:25600\n"
    "v_mfma_f32_16x16x32_bf16 v[76:79], v[48:51], v[196:199], v[76:79]\n"
    "s_waitcnt lgkmcnt(6)\n"
    "v_mfma_f32_16x16x32_bf16 v[80:83], v[52:55], v[192:195], v[80:83]\n"
    "ds_read_b64_tr_b16 v[48:49], v42 offset:24576\n"
    "ds_read_b64_tr_b16 v[50:51], v42 offset:25600\n"
    "v_mfma_f32_16x16x32_bf16 v[84:87], v[52:55], v[196:199], v[84:87]\n"
    "s_waitcnt lgkmcnt(6)\n"
    "v_mfma_f32_16x16x32_bf16 v[88:91], v[56:59], v[192:195], v[88:91]\n"
    "ds_read_b64_tr_b16 v[52:53], v43 offset:24576\n"
    "ds_read_b64_tr_b16 v[54:55], v43 offset:25600\n"
    "v_mfma_f32_16x16x32_bf16 v[92:95], v[56:59], v[196:199], v[92:95]\n"
    "s_waitcnt lgkmcnt(6)\n"
    "v_mfma_f32_16x16x32_bf16 v[32:35], v[36:39], v[60:63], v[32:35]\n"
    "v_mfma_f32_16x16x32_bf16 v[64:67], v[60:63], v[200:203], v[64:67]\n"
    "ds_read_b64_tr_b16 v[56:57], v40 offset:32768\n"
    "ds_read_b64_tr_b16 v[58:59], v40 offset:33792\n"
    "v_mfma_f32_16x16x32_bf16 v[68:71], v[60:63], v[204:207], v[68:71]\n"
    "s_waitcnt lgkmcnt(6)\n"
    "v_mfma_f32_16x16x32_bf16 v[72:75], v[44:47], v[200:203], v[72:75]\n"
    "ds_read_b64_tr_b16 v[60:61], v41 offset:32768\n"
    "ds_read_b64_tr_b16 v[62:63], v41 offset:33792\n"
    "v_mfma_f32_16x16x32_bf16 v[76:79], v[44:47], v[204:207], v[76:79]\n"
    "s_waitcnt lgkmcnt(6)\n"
    "v_mfma_f32_16x16x32_bf16 v[80:83], v[48:51], v[200:203], v[80:83]\n"
    "ds_read_b64_tr_b16 v[44:45], v42 offset:32768\n"
    "ds_read_b64_tr_b16 v[46:47], v42 offset:33792\n"
    "v_mfma_f32_16x16x32_bf16 v[84:87], v[48:51], v[204:207], v[84:87]\n"
    "s_waitcnt lgkmcnt(6)\n"
    "v_mfma_f32_16x16x32_bf16 v[88:91], v[52:55], v[200:203], v[88:91]\n"
    "ds_read_b64_tr_b16 v[48:49], v43 offset:32768\n"
    "ds_read_b64_tr_b16 v[50:51], v43 offset:33792\n"
    "v_mfma_f32_16x16x32_bf16 v[92:95], v[52:55], v[204:207], v[92:95]\n"
    "s_waitcnt lgkmcnt(6)\n"
    "v_mfma_f32_16x16x32_bf16 v[32:35], v[36:39], v[56:59], v[32:35]\n"
    "v_mfma_f32_16x16x32_bf16 v[64:67], v[56:59], v[208:211], v[64:67]\n"
    "ds_read_b64_tr_b16 v[52:53], v40 offset:40960\n"
    "ds_read_b64_tr_b16 v[54:55], v40 offset:41984\n"
    "v_mfma_f32_16x16x32_bf16 v[68:71], v[56:59], v[212:215], v[68:71]\n"
    "s_waitcnt lgkmcnt(6)\n"
    "v_mfma_f32_16x16x32_bf16 v[72:75], v[60:63], v[208:211], v[72:75]\n"
    "ds_read_b64_tr_b16 v[56:57], v41 offset:40960\n"
    "ds_read_b64_tr_b16 v[58:59], v41 offset:41984\n"
    "v_mfma_f32_16x16x32_bf16 v[76:79], v[60:63], v[212:215], v[76:79]\n"
    "s_waitcnt lgkmcnt(6)\n"
    "v_mfma_f32_16x16x32_bf16 v[80:83], v[44:47], v[208:211], v[80:83]\n"
    "ds_read_b64_tr_b16 v[60:61], v42 offset:40960\n"
    "ds_read_b64_tr_b16 v[62:63], v42 offset:41984\n"
    "v_mfma_f32_16x16x32_bf16 v[84:87], v[44:47], v[212:215], v[84:87]\n"
    "s_waitcnt lgkmcnt(6)\n"
    "v_mfma_f32_16x16x32_bf16 v[88:91], v[48:51], v[208:211], v[88:91]\n"
    "ds_read_b64_tr_b16 v[44:45], v43 offset:40960\n"
    "ds_read_b64_tr_b16 v[46:47], v43 offset:41984\n"
    "v_mfma_f32_16x16x32_bf16 v[92:95], v[48:51], v[212:215], v[92:95]\n"
    "s_waitcnt lgkmcnt(6)\n"
    "v_mfma_f32_16x16x32_bf16 v[32:35], v[36:39], v[52:55], v[32:35]\n"
    "v_mfma_f32_16x16x32_bf16 v[64:67], v[52:55], v[216:219], v[64:67]\n"
    "ds_read_b64_tr_b16 v[48:49], v40 offset:49152\n"
    "ds_read_b64_tr_b16 v[50:51], v40 offset:50176\n"
    "v_mfma_f32_16x16x32_bf16 v[68:71], v[52:55], v[220:223], v[68:71]\n"
    "s_waitcnt lgkmcnt(6)\n"
    "v_mfma_f32_16x16x32_bf16 v[72:75], v[56:59], v[216:219], v[72:75]\n"
    "ds_read_b64_tr_b16 v[52:53], v41 offset:49152\n"
    "ds_read_b64_tr_b16 v[54:55], v41 offset:50176\n"
    "v_mfma_f32_16x16x32_bf16 v[76:79], v[56:59], v[220:223], v[76:79]\n"
    "s_waitcnt lgkmcnt(6)\n"
    "v_mfma_f32_16x16x32_bf16 v[80:83], v[60:63], v[216:219], v[80:83]\n"
    "ds_read_b64_tr_b16 v[56:57], v42 offset:49152\n"
    "ds_read_b64_tr_b16 v[58:59], v42 offset:50176\n"
    "v_mfma_f32_16x16x32_bf16 v[84:87], v[60:63], v[220:223], v[84:87]\n"
    "s_waitcnt lgkmcnt(6)\n"
    "v_mfma_f32_16x16x32_bf16 v[88:91], v[44:47], v[216:219], v[88:91]\n"
    "ds_read_b64_tr_b16 v[60:61], v43 offset:49152\n"
    "ds_read_b64_tr_b16 v[62:63], v43 offset:50176\n"
    "v_mfma_f32_16x16x32_bf16 v[92:95], v[44:47], v[220:223], v[92:95]\n"
    "s_waitcnt lgkmcnt(6)\n"
    "v_mfma_f32_16x16x32_bf16 v[32:35], v[36:39], v[48:51], v[32:35]\n"
    "v_mfma_f32_16x16x32_bf16 v[64:67], v[48:51], v[224:227], v[64:67]\n"
    "ds_read_b64_tr_b16 v[44:45], v40 offset:57344\n"
    "ds_read_b64_tr_b16 v[46:47], v40 offset:58368\n"
    "v_mfma_f32_16x16x32_bf16 v[68:71], v[48:51], v[228:231], v[68:71]\n"
    "s_waitcnt lgkmcnt(6)\n"
    "v_mfma_f32_16x16x32_bf16 v[72:75], v[52:55], v[224:227], v[72:75]\n"
    "ds_read_b64_tr_b16 v[48:49], v41 offset:57344\n"
    "ds_read_b64_tr_b16 v[50:51], v41 offset:58368\n"
    "v_mfma_f32_16x16x32_bf16 v[76:79], v[52:55], v[228:231], v[76:79]\n"
    "s_waitcnt lgkmcnt(6)\n"
    "v_mfma_f32_16x16x32_bf16 v[80:83], v[56:59], v[224:227], v[80:83]\n"
    "ds_read_b64_tr_b16 v[52:53], v42 offset:57344\n"
    "ds_read_b64_tr_b16 v[54:55], v42 offset:58368\n"
    "v_mfma_f32_16x16x32_bf16 v[84:87], v[56:59], v[228:231], v[84:87]\n"
    "s_waitcnt lgkmcnt(6)\n"
    "v_mfma_f32_16x16x32_bf16 v[88:91], v[60:63], v[224:227], v[88:91]\n"
    "ds_read_b64_tr_b16 v[56:57], v43 offset:57344\n"
    "ds_read_b64_tr_b16 v[58:59], v43 offset:58368\n"
    "v_mfma_f32_16x16x32_bf16 v[92:95], v[60:63], v[228:231], v[92:95]\n"
    "s_waitcnt lgkmcnt(6)\n"
    "v_mfma_f32_16x16x32_bf16 v[32:35], v[36:39], v[44:47], v[32:35]\n"
    "v_mfma_f32_16x16x32_bf16 v[64:67], v[44:47], v[232:235], v[64:67]\n"
    "v_add_u32 v40, 0x10000, v40\n"
    "v_add_u32 v41, 0x10000, v41\n"
    "v_add_u32 v42, 0x10000, v42\n"
    "v_add_u32 v43, 0x10000, v43\n"
    "ds_read_b64_tr_b16 v[60:61], v40 offset:0\n"
    "ds_read_b64_tr_b16 v[62:63], v40 offset:1024\n"
    "v_mfma_f32_16x16x32_bf16 v[68:71], v[44:47], v[236:239], v[68:71]\n"
    "s_waitcnt lgkmcnt(6)\n"
    "v_mfma_f32_16x16x32_bf16 v[72:75], v[48:51], v[232:235], v[72:75]\n"
    "ds_read_b64_tr_b16 v[44:45], v40 offset:8192\n"
    "ds_read_b64_tr_b16 v[46:47], v40 offset:9216\n"
    "v_mfma_f32_16x16x32_bf16 v[76:79], v[48:51], v[236:239], v[76:79]\n"
    "s_waitcnt lgkmcnt(6)\n"
    "v_mfma_f32_16x16x32_bf16 v[80:83], v[52:55], v[232:235], v[80:83]\n"
    "ds_read_b64_tr_b16 v[48:49], v41 offset:0\n"
    "ds_read_b64_tr_b16 v[50:51], v41 offset:1024\n"
    "v_mfma_f32_16x16x32_bf16 v[84:87], v[52:55], v[236:239], v[84:87]\n"
    "s_waitcnt lgkmcnt(6)\n"
    "v_mfma_f32_16x16x32_bf16 v[88:91], v[56:59], v[232:235], v[88:91]\n"
    "ds_read_b64_tr_b16 v[52:53], v42 offset:0\n"
    "ds_read_b64_tr_b16 v[54:55], v42 offset:1024\n"
    "v_mfma_f32_16x16x32_bf16 v[92:95], v[56:59], v[236:239], v[92:95]\n"
    "s_waitcnt lgkmcnt(6)\n"
    "v_mfma_f32_16x16x32_bf16 v[32:35], v[36:39], v[60:63], v[32:35]\n"
    "v_mfma_f32_16x16x32_bf16 v[64:67], v[60:63], v[240:243], v[64:67]\n"
    "ds_read_b64_tr_b16 v[56:57], v43 offset:0\n"
    "ds_read_b64_tr_b16 v[58:59], v43 offset:1024\n"
    "v_mfma_f32_16x16x32_bf16 v[68:71], v[60:63], v[244:247], v[68:71]\n"
    "s_waitcnt lgkmcnt(6)\n"
    "v_mfma_f32_16x16x32_bf16 v[32:35], v[36:39], v[44:47], v[32:35]\n"
    "v_mfma_f32_16x16x32_bf16 v[64:67], v[44:47], v[248:251], v[64:67]\n"
    "ds_read_b64_tr_b16 v[60:61], v41 offset:8192\n"
    "ds_read_b64_tr_b16 v[62:63], v41 offset:9216\n"
    "v_mfma_f32_16x16x32_bf16 v[68:71], v[44:47], v[252:255], v[68:71]\n"
    "s_waitcnt lgkmcnt(6)\n"
    "v_mfma_f32_16x16x32_bf16 v[72:75], v[48:51], v[240:243], v[72:75]\n"
    "ds_read_b64_tr_b16 v[44:45], v42 offset:8192\n"
    "ds_read_b64_tr_b16 v[46:47], v42 offset:9216\n"
    "v_mfma_f32_16x16x32_bf16 v[76:79], v[48:51], v[244:247], v[76:79]\n"
    "s_nop 1\n"
    "v_cvt_pk_bf16_f32 v33, v32, v32\n"
    "v_lshlrev_b32 v33, 16, v33\n"
    "v_sub_f32 v33, v32, v33\n"
    "v_cvt_pk_bf16_f32 v32, v32, v33\n"
    "ds_write_b32 v21, v32\n"
    "ds_add_u32 v120, v121\n"
    "s_waitcnt lgkmcnt(8)\n"
    "v_mfma_f32_16x16x32_bf16 v[80:83], v[52:55], v[240:243], v[80:83]\n"
    "ds_read_b64_tr_b16 v[48:49], v43 offset:8192\n"
    "ds_read_b64_tr_b16 v[50:51], v43 offset:9216\n"
    "v_mfma_f32_16x16x32_bf16 v[84:87], v[52:55], v[244:247], v[84:87]\n"
    "s_waitcnt lgkmcnt(8)\n"
    "v_mfma_f32_16x16x32_bf16 v[88:91], v[56:59], v[240:243], v[88:91]\n"
    "v_mfma_f32_16x16x32_bf16 v[92:95], v[56:59], v[244:247], v[92:95]\n"
    "v_xor_b32 v32, 0, v21\n"
    "v_xor_b32 v33, 64, v21\n"
    "v_xor_b32 v34, 128, v21\n"
    "v_xor_b32 v35, 192, v21\n"
    "ds_read_b32 v122, v120\n"
    "ds_read_b32 v104, v32\n"
    "ds_read_b32 v106, v33\n"
    "ds_read_b32 v108, v34\n"
    "ds_read_b32 v110, v35\n"
    "s_waitcnt lgkmcnt(11)\n"
    "v_mfma_f32_16x16x32_bf16 v[72:75], v[60:63], v[248:251], v[72:75]\n"
    "v_mfma_f32_16x16x32_bf16 v[76:79], v[60:63], v[252:255], v[76:79]\n"
    "s_waitcnt lgkmcnt(9)\n"
    "v_mfma_f32_16x16x32_bf16 v[80:83], v[44:47], v[248:251], v[80:83]\n"
    "v_mfma_f32_16x16x32_bf16 v[84:87], v[44:47], v[252:255], v[84:87]\n"
    "s_waitcnt lgkmcnt(5)\n"
    "v_mfma_f32_16x16x32_bf16 v[88:91], v[48:51], v[248:251], v[88:91]\n"
    "v_mfma_f32_16x16x32_bf16 v[92:95], v[48:51], v[252:255], v[92:95]\n"
    "s_waitcnt vmcnt(0) lgkmcnt(0)\n"
    "3:\n"
    "v_cmp_ne_u32 vcc, 4, v122\n"
    "s_cbranch_vccz 4f\n"
    "ds_read_b32 v122, v120\n"
    "ds_read_b32 v104, v32\n"
    "ds_read_b32 v106, v33\n"
    "ds_read_b32 v108, v34\n"
    "ds_read_b32 v110, v35\n"
    "s_waitcnt lgkmcnt(0)\n"
    "s_branch 3b\n"
    "4:\n"
    "v_mfma_f32_16x16x16_bf16 v[64:67], v[104:105], v[28:29], v[64:67]\n"
    "v_mfma_f32_16x16x16_bf16 v[72:75], v[106:107], v[28:29], v[72:75]\n"
    "v_mfma_f32_16x16x16_bf16 v[68:71], v[104:105], v[24:25], v[68:71]\n"
    "v_mfma_f32_16x16x16_bf16 v[76:79], v[106:107], v[24:25], v[76:79]\n"
    "v_mfma_f32_16x16x16_bf16 v[80:83], v[108:109], v[28:29], v[80:83]\n"
    "v_mfma_f32_16x16x16_bf16 v[88:91], v[110:111], v[28:29], v[88:91]\n"
    "v_mfma_f32_16x16x16_bf16 v[84:87], v[108:109], v[24:25], v[84:87]\n"
    "v_mfma_f32_16x16x16_bf16 v[92:95], v[110:111], v[24:25], v[92:95]\n"
    ".Lp1_e:\n"
    "s_nop 7\n"
      : "=&{v[64:95]}"(acc), "+{v[40:43]}"(wa), "+{v[28:31]}"(g0), "+{v[24:27]}"(g1), "=&{v[96:97]}"(o.tag), "=&{v[192:223]}"(o.q)
      : "{v6}"(lane * 16), "{s[40:47]}"(sp), "{s48}"(G.remoff), [srow] "s"(row * 1280), [rx] "s"(mk(x, 2048 * 1280)),
        [rq] "s"(mk(wqkv, 640 * 3840)), [r1] "s"(mk(w1, j == 14 ? 0 : 640 * 3456)), [rw] "s"(mk(ws, WS_END)), [rb] "s"(mk(b1, 3456))
      : "m0", "vcc", "scc", "memory",
        "s49", "s50", "s51", "s52", "s53", "s54", "s55", "s56", "s57", "s58", "s59", "s60", "s61", "s62", "s63", "s64",
        "s65", "s66", "v0", "v1", "v2", "v3", "v4", "v5", "v7", "v8", "v9", "v10", "v11", "v12", "v13", "v14",
        "v15", "v16", "v17", "v18", "v19", "v21", "v22", "v23", "v32", "v33", "v34", "v35", "v36", "v37", "v38", "v39",
        "v44", "v45", "v46", "v47", "v48", "v49", "v50", "v51", "v52", "v53", "v54", "v55", "v56", "v57", "v58", "v59",
        "v60", "v61", "v62", "v63", "v98", "v99", "v100", "v101", "v102", "v103", "v104", "v105", "v106", "v107", "v108", "v109",
        "v110", "v111", "v112", "v113", "v114", "v115", "v116", "v117", "v118", "v119", "v120", "v121", "v122", "v123", "v124", "v125",
        "v126", "v127", "v128", "v129", "v130", "v131", "v132", "v133", "v134", "v135", "v136", "v137", "v138", "v139", "v140", "v141",
        "v142", "v143", "v144", "v145", "v146", "v147", "v148", "v149", "v150", "v151", "v152", "v153", "v154", "v155", "v156", "v157",
        "v158", "v159", "v160", "v161", "v162", "v163", "v164", "v165", "v166", "v167", "v168", "v169", "v170", "v171", "v172", "v173",
        "v174", "v175", "v176", "v177", "v178", "v179", "v180", "v181", "v182", "v183", "v184", "v185", "v186", "v187", "v188", "v189",
        "v190", "v191", "v224", "v225", "v226", "v227", "v228", "v229", "v230", "v231", "v232", "v233", "v234", "v235", "v236", "v237",
        "v238", "v239", "v240", "v241", "v242", "v243", "v244", "v245", "v246", "v247", "v248", "v249", "v250", "v251", "v252", "v253",
        "v254", "v255"
  );
#pragma unroll
  for (int t = 0; t < 4; t++)
#pragma unroll
    for (int m = 0; m < 2; m++) o.v[t][m] = tile(acc, t, m);
  o.r[0] = __builtin_bit_cast(f4, g0)[3]; o.r[1] = __builtin_bit_cast(f4, g1)[3];
}

DEV void kv_dma(char* ws, const Geo& G, u32 lane, int i) {
  const u32 t = i * 4 + G.w - 4, sv = WS_V + t * 40960 + G.head * 320;
  dma_sc1(mk(ws, WS_END), (lane >> 5) * SLOT + (lane & 31) * 16, WS_QK + (80 + G.sa) * SLOT + t * 512, L_K + t * 1024);
  asm volatile("s_bfm_b64 exec, 40, 0\n\tbuffer_load_dwordx4 %1, %2, %3 offen sc1 lds\n\t"
               "buffer_load_dwordx4 %1, %2, %4 offen offset:640 sc1 lds\n\ts_mov_b64 exec, -1"
               ::"{m0}"(L_V + t * 1296), "v"(lane * 16 + (lane < 20 ? 0 : 9920)), "s"(mk(ws, WS_END)), "s"(sv), "s"(sv + 19840) : "memory");
}
DEV u32x2 rem_load(char* ws, const Geo& G, u32 lane, int i) { return ld64(mk(ws, WS_END), lane * 16, WS_QK + 80 * SLOT + G.remoff + i * 1024); }
DEV void rem_put(u32x2 d, u32 lane, int i) { *(LDSP(u32x2))(L_R + (i >> 1) * 1024 + lane * 16 + (i & 1) * 8) = d; }

DEV f4 gelu4(f4 a, float r) {
  constexpr float c0 = -2.f * 0.7978845608028654f * 1.4426950408889634f, c1 = 0.044715f * c0;
  const float k0 = c0 * r, k1 = c1 * r * r * r, ir = __builtin_amdgcn_rcpf(r);
  f4 u = a * __builtin_elementwise_fma(a * a, f4{k1, k1, k1, k1}, f4{k0, k0, k0, k0});
#pragma unroll
  for (int i = 0; i < 4; i++) u[i] = __builtin_amdgcn_exp2f(u[i]);
  u = __builtin_elementwise_fma(u, f4{ir, ir, ir, ir}, f4{ir, ir, ir, ir});
#pragma unroll
  for (int i = 0; i < 4; i++) u[i] = __builtin_amdgcn_rcpf(u[i]);
  return a * u;
}
DEV void h_store(u32x4 d, int i, char* ws, int w, u32 lane, int rb, int j) {
  const u32 g = lane >> 4;
  const u32 vo = (2 * ((2 * (i & 1) + (g & 1)) ^ w) + (g >> 1)) * SLOT + (lane & 15) * 16 + (i >> 1) * 256;
  st128(d, mk(ws + WS_H, j * 2 + (w >> 2) < 27 ? SZ_H : 0), vo, j * 16 * SLOT + rb * 2048 + (w & 3) * 512);
}

DEV void transpose_w(const void* x, const void* w2, const void* wo, char* ws, const Geo& G, u32 lane, P1Out& o) {
  const rsrc_t rw = mk(ws + WS_W, SZ_W);
  const int w = G.w, rb = G.rb;
  const u32 r4 = lane >> 4, h = lane >> 5, q = (lane & 15) >> 2;
  u64 tag;
  if (G.j == 14) {
    tag = uniform64(o.tag[0], o.tag[1]);
    wait_bar<0>();
    if (w == 0 && lane == 0) stflag(ws + F_QKV + (14 * 16 + rb) * 8, tag);
  } else {
    constexpr int NU = 6;
    const u32 vs = r4 * 1280 + (((lane & 15) ^ (r4 << 2)) << 4);
    const u32 ar = w * 20480 + (8 * h + q) * 256 + (q << 6) + ((lane >> 4) & 1) * 32 + (lane & 3) * 8;
    const u32 vd = (h * 640 + (lane & 31)) * 16, row = rb * 128 + 120 + w;
    auto issue = [&](int i) {
      const int u = rb * 8 + w + 128 * i, kb = u / 5, cb = u - kb * 5;
      const rsrc_t rs = mk(kb < 108 ? (const char*)w2 + kb * 20480 : (const char*)wo + (kb - 108) * 20480, u < 740 ? 20480 : 0);
#pragma unroll
      for (int k = 0; k < 4; k++) dma(rs, vs, k * 5120 + cb * 256, w * 20480 + i % 5 * 4096 + k * 1024);
    };
    u32x2 non;
    u32x4 ra;
    u32 rb1;
    asm volatile("buffer_load_dwordx4 %0, %3, %5, %6 offen\n\tbuffer_load_dword %1, %4, %5, %6 offen offset:1024\n\tbuffer_load_dwordx2 %2, off, %7, 0 sc1"
                 : "=&v"(ra), "=&v"(rb1), "=&v"(non)
                 : "v"(lane * 16), "v"(lane * 4), "s"(mk(x, 2048 * 1280)), "s"(row * 1280), "s"(mk(ws, WS_END)) : "memory");
    asm volatile("s_barrier" ::: "memory");
    u64 pc;
    u32 back, len, pf = (w * 64 + lane) * 128;
    asm volatile("s_getpc_b64 %0\n.Lfb_here:\n\ts_mov_b32 %1, .Lfb_here-.Lfb_code_begin\n\ts_mov_b32 %2, .Lfb_code_end-.Lfb_code_begin" : "=s"(pc), "=s"(back), "=s"(len));
    asm volatile("buffer_load_dword %0, %0, %1, 0 offen" : "+v"(pf) : "s"(mk((void*)(pc - back), len)) : "memory");
    issue(0);
    asm volatile("s_waitcnt vmcnt(5)" : "+v"(ra), "+v"(rb1), "+v"(non), "+v"(pf) : : "memory");
    tag = mix(uniform64(non[0], non[1]));
    row_stats(ra, rb1, ws, tag, lane, row);
    asm volatile("s_barrier" ::: "memory");
#pragma unroll
    for (int i = 1; i < TR_DEPTH; i++) issue(i);
    sfor<0, NU>([&](auto I) {
      constexpr int i = I;
      constexpr int st = i < TR_DEPTH - 1 ? i : TR_DEPTH - 1, ld = NU - 1 - i < TR_DEPTH - 1 ? NU - 1 - i : TR_DEPTH - 1;
      wait_vm<4 * (st + ld)>();
      if (i == 0) asm volatile("" :: "v"(pf));
      const int u = rb * 8 + w + 128 * i, kb = u / 5, cb = u - kb * 5;
      u32x4 f[4];
#pragma unroll
      for (int t = 0; t < 4; t++) f[t] = trfrag((ar ^ (t << 6)) + i % 5 * 4096);
#pragma unroll
      for (int t = 0; t < 4; t++) st128(f[t], rw, (u < 740 ? vd : OOB) + t * 512, kb * 20480 + cb * 2048);
      if (i + TR_DEPTH < NU) issue(i + TR_DEPTH);
    });
  }
  if (w == 0) {
    const u32x2 vpoll = qkv_flags(lane, G.head, G.qb);
    poll(ws + vpoll[0], tag); poll(ws + vpoll[1], tag);
  }
  o = P1Out{};
  o.tag = u32x2{(u32)tag, (u32)(tag >> 32)};
  if (w == 7) *(LDSP(u32))(L_CNT + lane * 4) = 4;
  if (w < 4) {
    wait_bar<0>();
    if (w == 0 && lane == 0) stflag(ws + F_QKV + ((G.j + 1) * 16 + rb) * 8, tag);
    const rsrc_t rk = mk(ws + WS_QK, SZ_QK);
    const u32 vq = h * SLOT + (lane & 31) * 16, qrow = (G.qb * 256 + w * 64) * 16;
    const u32x4 q0 = ld128(rk, vq, G.sa * SLOT + qrow), q1 = ld128(rk, vq + 512, G.sa * SLOT + qrow), k0 = ld128(rk, vq, (80 + G.sa) * SLOT);
    const u32x2 qr = ld64(rk, lane * 16, G.remoff + qrow), kr = rem_load(ws, G, lane, 0);
#pragma unroll
    for (int i = 0; i < 4; i++) { o.q[i] = q0[i]; o.q[4 + i] = q1[i]; o.q[8 + i] = qr[i & 1]; o.q[12 + i] = k0[i]; o.q[24 + i] = kr[i & 1]; }
    __builtin_amdgcn_s_waitcnt(0x0f70);
  }
}

DEV f32v mfma2b(u32x2 a, u32x2 b) { return __builtin_amdgcn_mfma_f32_32x32x4bf16_1k(__builtin_bit_cast(s16x4, a), __builtin_bit_cast(s16x4, b), f32v{}, 0, 0, 0); }
DEV void tile_max(const f32v& s, float& m0, float& m1) {
#pragma unroll
  for (int i = 0; i < 16; i += 8)
    asm("v_max3_f32 %0, %0, %2, %3\n\tv_max3_f32 %1, %1, %10, %11\n\tv_max3_f32 %0, %0, %4, %5\n\tv_max3_f32 %1, %1, %12, %13\n\t"
        "v_max3_f32 %0, %0, %6, %7\n\tv_max3_f32 %1, %1, %14, %15\n\tv_max3_f32 %0, %0, %8, %9\n\tv_max3_f32 %1, %1, %16, %17"
        : "+v"(m0), "+v"(m1)
        : "v"(s[i]), "v"(s[i + 1]), "v"(s[i + 2]), "v"(s[i + 3]), "v"(s[i + 4]), "v"(s[i + 5]), "v"(s[i + 6]), "v"(s[i + 7]), "v"(s[i + 16]),
          "v"(s[i + 17]), "v"(s[i + 18]), "v"(s[i + 19]), "v"(s[i + 20]), "v"(s[i + 21]), "v"(s[i + 22]), "v"(s[i + 23]));
}
DEV f32v neg_max(float m0, float m1) {
  asm("s_nop 1\n\tv_permlane32_swap_b32 %0, %1\n\tv_max_f32 %0, %0, %1" : "+v"(m0), "+v"(m1));
  const float r = m0 - bfhi(__builtin_bit_cast(u32, m0)), lo = r - bfhi(__builtin_bit_cast(u32, r));
  return mfma2b(u32x2{0xbf80bf80u, 0xbf80bf80u},
                u32x2{__builtin_amdgcn_perm(__builtin_bit_cast(u32, r), __builtin_bit_cast(u32, m0), 0x07060302u), __builtin_bit_cast(u32, lo) >> 16});
}
DEV f32v score_rem(u32x2 kr, u32x2 qr, f32v c) {
  return __builtin_amdgcn_mfma_f32_32x32x4bf16_1k(__builtin_bit_cast(s16x4, kr), __builtin_bit_cast(s16x4, qr), c, 1, 0, 0);
}
DEV f32v score_main(u32x4 kf, u32x4 q0, u32x4 q1, f32v s) {
  f16v a = __builtin_shufflevector(s, s, 0, 1, 2, 3, 4, 5, 6, 7, 8, 9, 10, 11, 12, 13, 14, 15);
  f16v b = __builtin_shufflevector(s, s, 16, 17, 18, 19, 20, 21, 22, 23, 24, 25, 26, 27, 28, 29, 30, 31);
  a = mfma32(kf, q0, a); b = mfma32(kf, q1, b);
  return __builtin_shufflevector(a, b, 0, 1, 2, 3, 4, 5, 6, 7, 8, 9, 10, 11, 12, 13, 14, 15, 16, 17, 18, 19, 20, 21, 22, 23, 24, 25, 26, 27, 28, 29, 30, 31);
}

DEV u32 row_chunk(int i, u32 lane) {
  const u32 c = i * 64 + lane;
  return (__umul24(__umul24(c, 6554) >> 16, 1120) + c * 16) | (i == 2 ? lane << 26 & OOB : 0);
}
DEV void attention_waves(void* out, char* ws, const Geo& G, const P1Out& p1) {
  const rsrc_t rk = mk(ws + WS_QK, SZ_QK);
  const int w = G.w;
  const u32 qrow = (G.qb * 256 + w * 64) * 16, krem = 80 * SLOT + G.remoff;
  u32 lane = lane_id();
  f4 hv[2];
  auto h_part = [&](int i) {
    __builtin_amdgcn_sched_barrier(0);
    hv[i & 1] = gelu4(p1.v[i & 3][i >> 2], p1.r[i >> 2]);
    if (i & 1) h_store(pair8(hv[0], hv[1]), i >> 1, ws, w, lane, G.rb, G.j);
    else keep(__builtin_bit_cast(u32x4, hv[0]));
    __builtin_amdgcn_sched_barrier(0);
  };
  auto zeros = [] {
    u32x2 z = {};
    asm volatile("" : "+v"(z));
    f32v r = mfma2b(z, z);
    asm volatile("" : "+{v[160:191]}"(r));
    return r;
  };
  f32v o = zeros();
  u32x2 rr[3];
#pragma unroll
  for (int i = 0; i < SP; i++) {
    if (i == 1)
#pragma unroll
      for (int e = 0; e < 3; e++) rr[e] = rem_load(ws, G, lane, e + 1);
    h_part(i);
  }
  u32 a0, a1, aw, ak, ar, idx;
  auto prepare = [&] {
    const u32 dch = __builtin_amdgcn_ubfe(G.head & 1 ? 0x04231 : 0x43120, lane & 28, 3) + (lane >> 5) * 5;
    const u32 av = L_V + (dch * 4 + (lane & 3)) * 16, pad = (lane & 27) == 24 ? L_V + 1280 : 0x40000000u;
    const bool in = (lane & 31) < 20;
    a0 = in ? av : pad; a1 = in ? av + 640 : pad;
    aw = ((lane >> 4) * 624 + lane) * 16;
    ak = L_K + lane * 16; ar = L_R + lane * 16; idx = 0xddddddddu;
    asm volatile("" : "+{v224}"(ak), "+{v225}"(a0), "+{v226}"(a1), "+{v227}"(ar), "+{v228}"(idx), "+{v229}"(aw));
  };
  prepare();
  u32 swo = (54 + w * 5) * 40960 + G.qb * 1280, so0 = SZ_W + G.sa * SLOT + qrow, so1 = SZ_W + G.remoff + qrow;
  rsrc_t rw = mk(ws + WS_W, SZ_W + SZ_O);
  asm volatile("" : "+s"(swo), "+s"(so0), "+s"(so1), "+s"(rw));
  u32x32 pq = p1.q;
  asm volatile("s_waitcnt vmcnt(%1)" : "+{v[192:223]}"(pq) : "i"(SP / 2 + 3));
  const u32x4 q0 = {pq[0], pq[1], pq[2], pq[3]}, q1 = {pq[4], pq[5], pq[6], pq[7]}, k0 = {pq[12], pq[13], pq[14], pq[15]};
  const u32x2 qr = {pq[8], pq[9]}, kr = {pq[24], pq[25]};
  u32x4 r0, r1;
  f32v s, neg;
  u32x32 wa, wb;
  u32x16 wc;
  u32x8 wd;
#define ATT_INOUT                                                                                                              \
  "+{v[96:127]}"(s), "+{v[128:159]}"(neg), "+{v[160:191]}"(o), "+{v[216:219]}"(r0), "+{v[220:223]}"(r1), "+{v224}"(ak),        \
      "+{v225}"(a0), "+{v226}"(a1), "+{v227}"(ar), "+{v228}"(idx), "+{v229}"(aw)
  auto run = [&](bool first) {
    u32 cnt;
    asm volatile(
    "ds_read_b128 v[208:211], v225\n"
    "ds_read_b128 v[204:207], v224 offset:1024\n"
    "s_mov_b32 %[cnt], 7\n"
    "v_exp_f32 v96, v96\n"
    "v_exp_f32 v97, v97\n"
    "s_branch 5f\n"
    "8:\n"
    "s_mov_b32 %[cnt], 1\n"
    "s_waitcnt vmcnt(0)\n"
    "s_branch 9f\n"
    "2:\n"
    "s_waitcnt vmcnt(0)\n"
    "s_barrier\n"
    "s_branch 3f\n"
    "1:\n"
    "v_smfmac_f32_32x32x32_bf16 v[176:191], v[212:215], v[88:95], v228\n"
    "5:\n"
    "s_waitcnt lgkmcnt(1)\n"
    "ds_read_b128 v[212:215], v226 offset:0\n"
    "v_exp_f32 v98, v98\n"
    "v_exp_f32 v99, v99\n"
    "v_exp_f32 v100, v100\n"
    "v_exp_f32 v101, v101\n"
    "v_mfma_f32_32x32x4_2b_bf16 v[64:95], v[216:217], v[200:201], v[128:159] cbsz:1 abid:1\n"
    "v_exp_f32 v102, v102\n"
    "v_exp_f32 v103, v103\n"
    "v_exp_f32 v104, v104\n"
    "v_exp_f32 v105, v105\n"
    "v_exp_f32 v106, v106\n"
    "v_exp_f32 v107, v107\n"
    "v_exp_f32 v108, v108\n"
    "v_exp_f32 v109, v109\n"
    "v_smfmac_f32_32x32x32_bf16 v[160:175], v[208:211], v[96:103], v228\n"
    "s_waitcnt lgkmcnt(0)\n"
    "v_exp_f32 v110, v110\n"
    "v_exp_f32 v111, v111\n"
    "v_exp_f32 v112, v112\n"
    "v_exp_f32 v113, v113\n"
    "v_smfmac_f32_32x32x32_bf16 v[160:175], v[212:215], v[104:111], v228\n"
    "v_exp_f32 v114, v114\n"
    "v_exp_f32 v115, v115\n"
    "v_exp_f32 v116, v116\n"
    "v_exp_f32 v117, v117\n"
    "v_mfma_f32_32x32x16_bf16 v[64:79], v[204:207], v[192:195], v[64:79]\n"
    "v_exp_f32 v118, v118\n"
    "v_exp_f32 v119, v119\n"
    "v_exp_f32 v120, v120\n"
    "v_exp_f32 v121, v121\n"
    "v_smfmac_f32_32x32x32_bf16 v[176:191], v[208:211], v[112:119], v228\n"
    "ds_read_b128 v[208:211], v225 offset:1296\n"
    "v_exp_f32 v122, v122\n"
    "v_exp_f32 v123, v123\n"
    "v_exp_f32 v124, v124\n"
    "v_exp_f32 v125, v125\n"
    "v_mfma_f32_32x32x16_bf16 v[80:95], v[204:207], v[196:199], v[80:95]\n"
    "ds_read_b128 v[204:207], v224 offset:2048\n"
    "v_exp_f32 v126, v126\n"
    "v_exp_f32 v127, v127\n"
    "v_exp_f32 v64, v64\n"
    "v_exp_f32 v65, v65\n"
    "v_smfmac_f32_32x32x32_bf16 v[176:191], v[212:215], v[120:127], v228\n"
    "s_waitcnt lgkmcnt(1)\n"
    "ds_read_b128 v[212:215], v226 offset:1296\n"
    "v_exp_f32 v66, v66\n"
    "v_exp_f32 v67, v67\n"
    "v_exp_f32 v68, v68\n"
    "v_exp_f32 v69, v69\n"
    "v_mfma_f32_32x32x4_2b_bf16 v[96:127], v[218:219], v[200:201], v[128:159] cbsz:1 abid:0\n"
    "v_exp_f32 v70, v70\n"
    "v_exp_f32 v71, v71\n"
    "v_exp_f32 v72, v72\n"
    "v_exp_f32 v73, v73\n"
    "v_exp_f32 v74, v74\n"
    "v_exp_f32 v75, v75\n"
    "v_exp_f32 v76, v76\n"
    "v_exp_f32 v77, v77\n"
    "v_smfmac_f32_32x32x32_bf16 v[160:175], v[208:211], v[64:71], v228\n"
    "s_waitcnt lgkmcnt(0)\n"
    "v_exp_f32 v78, v78\n"
    "v_exp_f32 v79, v79\n"
    "v_exp_f32 v80, v80\n"
    "v_exp_f32 v81, v81\n"
    "v_smfmac_f32_32x32x32_bf16 v[160:175], v[212:215], v[72:79], v228\n"
    "v_exp_f32 v82, v82\n"
    "v_exp_f32 v83, v83\n"
    "v_exp_f32 v84, v84\n"
    "v_exp_f32 v85, v85\n"
    "v_mfma_f32_32x32x16_bf16 v[96:111], v[204:207], v[192:195], v[96:111]\n"
    "v_exp_f32 v86, v86\n"
    "v_exp_f32 v87, v87\n"
    "v_exp_f32 v88, v88\n"
    "v_exp_f32 v89, v89\n"
    "v_smfmac_f32_32x32x32_bf16 v[176:191], v[208:211], v[80:87], v228\n"
    "ds_read_b128 v[208:211], v225 offset:2592\n"
    "v_exp_f32 v90, v90\n"
    "v_exp_f32 v91, v91\n"
    "v_exp_f32 v92, v92\n"
    "v_exp_f32 v93, v93\n"
    "v_mfma_f32_32x32x16_bf16 v[112:127], v[204:207], v[196:199], v[112:127]\n"
    "ds_read_b128 v[204:207], v224 offset:3072\n"
    "v_exp_f32 v94, v94\n"
    "v_exp_f32 v95, v95\n"
    "v_exp_f32 v96, v96\n"
    "v_exp_f32 v97, v97\n"
    "v_smfmac_f32_32x32x32_bf16 v[176:191], v[212:215], v[88:95], v228\n"
    "s_waitcnt lgkmcnt(1)\n"
    "ds_read_b128 v[212:215], v226 offset:2592\n"
    "v_exp_f32 v98, v98\n"
    "v_exp_f32 v99, v99\n"
    "v_exp_f32 v100, v100\n"
    "v_exp_f32 v101, v101\n"
    "v_mfma_f32_32x32x4_2b_bf16 v[64:95], v[218:219], v[200:201], v[128:159] cbsz:1 abid:1\n"
    "v_exp_f32 v102, v102\n"
    "v_exp_f32 v103, v103\n"
    "v_exp_f32 v104, v104\n"
    "v_exp_f32 v105, v105\n"
    "v_exp_f32 v106, v106\n"
    "v_exp_f32 v107, v107\n"
    "v_exp_f32 v108, v108\n"
    "v_exp_f32 v109, v109\n"
    "v_smfmac_f32_32x32x32_bf16 v[160:175], v[208:211], v[96:103], v228\n"
    "s_waitcnt lgkmcnt(0)\n"
    "v_exp_f32 v110, v110\n"
    "v_exp_f32 v111, v111\n"
    "v_exp_f32 v112, v112\n"
    "v_exp_f32 v113, v113\n"
    "v_smfmac_f32_32x32x32_bf16 v[160:175], v[212:215], v[104:111], v228\n"
    "ds_read_b128 v[216:219], v227 offset:0\n"
    "v_exp_f32 v114, v114\n"
    "v_exp_f32 v115, v115\n"
    "v_exp_f32 v116, v116\n"
    "v_exp_f32 v117, v117\n"
    "v_mfma_f32_32x32x16_bf16 v[64:79], v[204:207], v[192:195], v[64:79]\n"
    "v_exp_f32 v118, v118\n"
    "v_exp_f32 v119, v119\n"
    "v_exp_f32 v120, v120\n"
    "v_exp_f32 v121, v121\n"
    "v_smfmac_f32_32x32x32_bf16 v[176:191], v[208:211], v[112:119], v228\n"
    "ds_read_b128 v[208:211], v225 offset:3888\n"
    "v_exp_f32 v122, v122\n"
    "v_exp_f32 v123, v123\n"
    "v_exp_f32 v124, v124\n"
    "v_exp_f32 v125, v125\n"
    "v_mfma_f32_32x32x16_bf16 v[80:95], v[204:207], v[196:199], v[80:95]\n"
    "ds_read_b128 v[204:207], v224 offset:4096\n"
    "v_exp_f32 v126, v126\n"
    "v_exp_f32 v127, v127\n"
    "v_exp_f32 v64, v64\n"
    "v_exp_f32 v65, v65\n"
    "v_smfmac_f32_32x32x32_bf16 v[176:191], v[212:215], v[120:127], v228\n"
    "s_waitcnt lgkmcnt(1)\n"
    "ds_read_b128 v[212:215], v226 offset:3888\n"
    "v_exp_f32 v66, v66\n"
    "v_exp_f32 v67, v67\n"
    "v_exp_f32 v68, v68\n"
    "v_exp_f32 v69, v69\n"
    "v_mfma_f32_32x32x4_2b_bf16 v[96:127], v[220:221], v[200:201], v[128:159] cbsz:1 abid:0\n"
    "v_exp_f32 v70, v70\n"
    "v_exp_f32 v71, v71\n"
    "v_exp_f32 v72, v72\n"
    "v_exp_f32 v73, v73\n"
    "v_exp_f32 v74, v74\n"
    "v_exp_f32 v75, v75\n"
    "v_exp_f32 v76, v76\n"
    "v_exp_f32 v77, v77\n"
    "v_smfmac_f32_32x32x32_bf16 v[160:175], v[208:211], v[64:71], v228\n"
    "s_waitcnt lgkmcnt(0)\n"
    "v_exp_f32 v78, v78\n"
    "v_exp_f32 v79, v79\n"
    "v_exp_f32 v80, v80\n"
    "v_exp_f32 v81, v81\n"
    "v_smfmac_f32_32x32x32_bf16 v[160:175], v[212:215], v[72:79], v228\n"
    "v_exp_f32 v82, v82\n"
    "v_exp_f32 v83, v83\n"
    "v_exp_f32 v84, v84\n"
    "v_exp_f32 v85, v85\n"
    "v_mfma_f32_32x32x16_bf16 v[96:111], v[204:207], v[192:195], v[96:111]\n"
    "v_exp_f32 v86, v86\n"
    "v_exp_f32 v87, v87\n"
    "v_exp_f32 v88, v88\n"
    "v_exp_f32 v89, v89\n"
    "v_smfmac_f32_32x32x32_bf16 v[176:191], v[208:211], v[80:87], v228\n"
    "ds_read_b128 v[208:211], v225 offset:5184\n"
    "v_exp_f32 v90, v90\n"
    "v_exp_f32 v91, v91\n"
    "v_exp_f32 v92, v92\n"
    "v_exp_f32 v93, v93\n"
    "v_mfma_f32_32x32x16_bf16 v[112:127], v[204:207], v[196:199], v[112:127]\n"
    "ds_read_b128 v[204:207], v224 offset:5120\n"
    "v_exp_f32 v94, v94\n"
    "v_exp_f32 v95, v95\n"
    "v_exp_f32 v96, v96\n"
    "v_exp_f32 v97, v97\n"
    "v_smfmac_f32_32x32x32_bf16 v[176:191], v[212:215], v[88:95], v228\n"
    "s_waitcnt lgkmcnt(1)\n"
    "ds_read_b128 v[212:215], v226 offset:5184\n"
    "v_exp_f32 v98, v98\n"
    "v_exp_f32 v99, v99\n"
    "v_exp_f32 v100, v100\n"
    "v_exp_f32 v101, v101\n"
    "v_mfma_f32_32x32x4_2b_bf16 v[64:95], v[220:221], v[200:201], v[128:159] cbsz:1 abid:1\n"
    "v_exp_f32 v102, v102\n"
    "v_exp_f32 v103, v103\n"
    "v_exp_f32 v104, v104\n"
    "v_exp_f32 v105, v105\n"
    "v_exp_f32 v106, v106\n"
    "v_exp_f32 v107, v107\n"
    "v_exp_f32 v108, v108\n"
    "v_exp_f32 v109, v109\n"
    "v_smfmac_f32_32x32x32_bf16 v[160:175], v[208:211], v[96:103], v228\n"
    "s_waitcnt lgkmcnt(0)\n"
    "v_exp_f32 v110, v110\n"
    "v_exp_f32 v111, v111\n"
    "v_exp_f32 v112, v112\n"
    "v_exp_f32 v113, v113\n"
    "v_smfmac_f32_32x32x32_bf16 v[160:175], v[212:215], v[104:111], v228\n"
    "v_exp_f32 v114, v114\n"
    "v_exp_f32 v115, v115\n"
    "v_exp_f32 v116, v116\n"
    "v_exp_f32 v117, v117\n"
    "v_mfma_f32_32x32x16_bf16 v[64:79], v[204:207], v[192:195], v[64:79]\n"
    "v_exp_f32 v118, v118\n"
    "v_exp_f32 v119, v119\n"
    "v_exp_f32 v120, v120\n"
    "v_exp_f32 v121, v121\n"
    "v_smfmac_f32_32x32x32_bf16 v[176:191], v[208:211], v[112:119], v228\n"
    "ds_read_b128 v[208:211], v225 offset:6480\n"
    "v_exp_f32 v122, v122\n"
    "v_exp_f32 v123, v123\n"
    "v_exp_f32 v124, v124\n"
    "v_exp_f32 v125, v125\n"
    "v_mfma_f32_32x32x16_bf16 v[80:95], v[204:207], v[196:199], v[80:95]\n"
    "ds_read_b128 v[204:207], v224 offset:6144\n"
    "v_exp_f32 v126, v126\n"
    "v_exp_f32 v127, v127\n"
    "v_exp_f32 v64, v64\n"
    "v_exp_f32 v65, v65\n"
    "v_smfmac_f32_32x32x32_bf16 v[176:191], v[212:215], v[120:127], v228\n"
    "s_waitcnt lgkmcnt(1)\n"
    "ds_read_b128 v[212:215], v226 offset:6480\n"
    "v_exp_f32 v66, v66\n"
    "v_exp_f32 v67, v67\n"
    "v_exp_f32 v68, v68\n"
    "v_exp_f32 v69, v69\n"
    "v_mfma_f32_32x32x4_2b_bf16 v[96:127], v[222:223], v[200:201], v[128:159] cbsz:1 abid:0\n"
    "v_exp_f32 v70, v70\n"
    "v_exp_f32 v71, v71\n"
    "v_exp_f32 v72, v72\n"
    "v_exp_f32 v73, v73\n"
    "v_exp_f32 v74, v74\n"
    "v_exp_f32 v75, v75\n"
    "v_exp_f32 v76, v76\n"
    "v_exp_f32 v77, v77\n"
    "v_smfmac_f32_32x32x32_bf16 v[160:175], v[208:211], v[64:71], v228\n"
    "s_waitcnt lgkmcnt(0)\n"
    "v_exp_f32 v78, v78\n"
    "v_exp_f32 v79, v79\n"
    "v_exp_f32 v80, v80\n"
    "v_exp_f32 v81, v81\n"
    "v_smfmac_f32_32x32x32_bf16 v[160:175], v[212:215], v[72:79], v228\n"
    "v_exp_f32 v82, v82\n"
    "v_exp_f32 v83, v83\n"
    "v_exp_f32 v84, v84\n"
    "v_exp_f32 v85, v85\n"
    "v_mfma_f32_32x32x16_bf16 v[96:111], v[204:207], v[192:195], v[96:111]\n"
    "v_exp_f32 v86, v86\n"
    "v_exp_f32 v87, v87\n"
    "v_exp_f32 v88, v88\n"
    "v_exp_f32 v89, v89\n"
    "v_smfmac_f32_32x32x32_bf16 v[176:191], v[208:211], v[80:87], v228\n"
    "ds_read_b128 v[208:211], v225 offset:7776\n"
    "v_exp_f32 v90, v90\n"
    "v_exp_f32 v91, v91\n"
    "v_exp_f32 v92, v92\n"
    "v_exp_f32 v93, v93\n"
    "v_mfma_f32_32x32x16_bf16 v[112:127], v[204:207], v[196:199], v[112:127]\n"
    "ds_read_b128 v[204:207], v224 offset:7168\n"
    "v_exp_f32 v94, v94\n"
    "v_exp_f32 v95, v95\n"
    "v_exp_f32 v96, v96\n"
    "v_exp_f32 v97, v97\n"
    "v_smfmac_f32_32x32x32_bf16 v[176:191], v[212:215], v[88:95], v228\n"
    "s_waitcnt lgkmcnt(1)\n"
    "ds_read_b128 v[212:215], v226 offset:7776\n"
    "v_exp_f32 v98, v98\n"
    "v_exp_f32 v99, v99\n"
    "v_exp_f32 v100, v100\n"
    "v_exp_f32 v101, v101\n"
    "v_mfma_f32_32x32x4_2b_bf16 v[64:95], v[222:223], v[200:201], v[128:159] cbsz:1 abid:1\n"
    "v_exp_f32 v102, v102\n"
    "v_exp_f32 v103, v103\n"
    "v_exp_f32 v104, v104\n"
    "v_exp_f32 v105, v105\n"
    "v_exp_f32 v106, v106\n"
    "v_exp_f32 v107, v107\n"
    "v_exp_f32 v108, v108\n"
    "v_exp_f32 v109, v109\n"
    "v_smfmac_f32_32x32x32_bf16 v[160:175], v[208:211], v[96:103], v228\n"
    "s_waitcnt lgkmcnt(0)\n"
    "v_exp_f32 v110, v110\n"
    "v_exp_f32 v111, v111\n"
    "v_exp_f32 v112, v112\n"
    "v_exp_f32 v113, v113\n"
    "v_smfmac_f32_32x32x32_bf16 v[160:175], v[212:215], v[104:111], v228\n"
    "ds_read_b128 v[220:223], v227 offset:1024\n"
    "v_exp_f32 v114, v114\n"
    "v_exp_f32 v115, v115\n"
    "v_exp_f32 v116, v116\n"
    "v_exp_f32 v117, v117\n"
    "v_mfma_f32_32x32x16_bf16 v[64:79], v[204:207], v[192:195], v[64:79]\n"
    "v_exp_f32 v118, v118\n"
    "v_exp_f32 v119, v119\n"
    "v_exp_f32 v120, v120\n"
    "v_exp_f32 v121, v121\n"
    "v_smfmac_f32_32x32x32_bf16 v[176:191], v[208:211], v[112:119], v228\n"
    "ds_read_b128 v[208:211], v225 offset:9072\n"
    "v_exp_f32 v122, v122\n"
    "v_exp_f32 v123, v123\n"
    "v_exp_f32 v124, v124\n"
    "v_exp_f32 v125, v125\n"
    "v_mfma_f32_32x32x16_bf16 v[80:95], v[204:207], v[196:199], v[80:95]\n"
    "ds_read_b128 v[204:207], v224 offset:8192\n"
    "v_exp_f32 v126, v126\n"
    "v_exp_f32 v127, v127\n"
    "v_exp_f32 v64, v64\n"
    "v_exp_f32 v65, v65\n"
    "v_smfmac_f32_32x32x32_bf16 v[176:191], v[212:215], v[120:127], v228\n"
    "s_waitcnt lgkmcnt(1)\n"
    "ds_read_b128 v[212:215], v226 offset:9072\n"
    "v_exp_f32 v66, v66\n"
    "v_exp_f32 v67, v67\n"
    "v_exp_f32 v68, v68\n"
    "v_exp_f32 v69, v69\n"
    "v_mfma_f32_32x32x4_2b_bf16 v[96:127], v[216:217], v[200:201], v[128:159] cbsz:1 abid:0\n"
    "v_exp_f32 v70, v70\n"
    "v_exp_f32 v71, v71\n"
    "v_exp_f32 v72, v72\n"
    "v_exp_f32 v73, v73\n"
    "v_exp_f32 v74, v74\n"
    "v_exp_f32 v75, v75\n"
    "v_exp_f32 v76, v76\n"
    "v_exp_f32 v77, v77\n"
    "v_smfmac_f32_32x32x32_bf16 v[160:175], v[208:211], v[64:71], v228\n"
    "s_waitcnt lgkmcnt(0)\n"
    "v_exp_f32 v78, v78\n"
    "v_exp_f32 v79, v79\n"
    "v_exp_f32 v80, v80\n"
    "v_exp_f32 v81, v81\n"
    "v_smfmac_f32_32x32x32_bf16 v[160:175], v[212:215], v[72:79], v228\n"
    "v_exp_f32 v82, v82\n"
    "v_exp_f32 v83, v83\n"
    "v_exp_f32 v84, v84\n"
    "v_exp_f32 v85, v85\n"
    "v_mfma_f32_32x32x16_bf16 v[96:111], v[204:207], v[192:195], v[96:111]\n"
    "s_sub_u32 %[cnt], %[cnt], 1\n"
    "v_exp_f32 v86, v86\n"
    "v_exp_f32 v87, v87\n"
    "v_exp_f32 v88, v88\n"
    "v_exp_f32 v89, v89\n"
    "v_smfmac_f32_32x32x32_bf16 v[176:191], v[208:211], v[80:87], v228\n"
    "s_cmp_ge_u32 %[cnt], %[bar]\n"
    "s_cbranch_scc1 2b\n"
    "3:\n"
    "ds_read_b128 v[208:211], v225 offset:10368\n"
    "v_exp_f32 v90, v90\n"
    "v_exp_f32 v91, v91\n"
    "v_exp_f32 v92, v92\n"
    "v_exp_f32 v93, v93\n"
    "v_mfma_f32_32x32x16_bf16 v[112:127], v[204:207], v[196:199], v[112:127]\n"
    "ds_read_b128 v[204:207], v224 offset:9216\n"
    "v_exp_f32 v94, v94\n"
    "v_exp_f32 v95, v95\n"
    "v_add_u32 v224, 8192, v224\n"
    "v_add_u32 v225, 10368, v225\n"
    "v_add_u32 v226, 10368, v226\n"
    "v_add_u32 v227, 2048, v227\n"
    "s_cmp_lg_u32 %[cnt], 0\n"
    "v_exp_f32 v96, v96\n"
    "v_exp_f32 v97, v97\n"
    "s_cbranch_scc1 1b\n"
    "v_smfmac_f32_32x32x32_bf16 v[176:191], v[212:215], v[88:95], v228\n"
    "s_waitcnt lgkmcnt(1)\n"
    "ds_read_b128 v[212:215], v226 offset:0\n"
    "v_exp_f32 v98, v98\n"
    "v_exp_f32 v99, v99\n"
    "v_exp_f32 v100, v100\n"
    "v_exp_f32 v101, v101\n"
    "v_mfma_f32_32x32x4_2b_bf16 v[64:95], v[216:217], v[200:201], v[128:159] cbsz:1 abid:1\n"
    "v_exp_f32 v102, v102\n"
    "v_exp_f32 v103, v103\n"
    "s_add_u32 %[cnt], %[wo], 0\n"
    "buffer_load_dwordx4 v[0:3], v229, %[rw], %[cnt] offen offset:0 sc1\n"
    "v_exp_f32 v104, v104\n"
    "v_exp_f32 v105, v105\n"
    "v_exp_f32 v106, v106\n"
    "v_exp_f32 v107, v107\n"
    "buffer_load_dwordx4 v[4:7], v229, %[rw], %[cnt] offen offset:256 sc1\n"
    "v_exp_f32 v108, v108\n"
    "v_exp_f32 v109, v109\n"
    "v_smfmac_f32_32x32x32_bf16 v[160:175], v[208:211], v[96:103], v228\n"
    "s_waitcnt lgkmcnt(0)\n"
    "v_exp_f32 v110, v110\n"
    "v_exp_f32 v111, v111\n"
    "v_exp_f32 v112, v112\n"
    "v_exp_f32 v113, v113\n"
    "v_smfmac_f32_32x32x32_bf16 v[160:175], v[212:215], v[104:111], v228\n"
    "buffer_load_dwordx4 v[8:11], v229, %[rw], %[cnt] offen offset:512 sc1\n"
    "v_exp_f32 v114, v114\n"
    "v_exp_f32 v115, v115\n"
    "v_exp_f32 v116, v116\n"
    "v_exp_f32 v117, v117\n"
    "v_mfma_f32_32x32x16_bf16 v[64:79], v[204:207], v[192:195], v[64:79]\n"
    "buffer_load_dwordx4 v[12:15], v229, %[rw], %[cnt] offen offset:768 sc1\n"
    "v_exp_f32 v118, v118\n"
    "v_exp_f32 v119, v119\n"
    "v_exp_f32 v120, v120\n"
    "v_exp_f32 v121, v121\n"
    "v_smfmac_f32_32x32x32_bf16 v[176:191], v[208:211], v[112:119], v228\n"
    "ds_read_b128 v[208:211], v225 offset:1296\n"
    "v_exp_f32 v122, v122\n"
    "v_exp_f32 v123, v123\n"
    "v_exp_f32 v124, v124\n"
    "v_exp_f32 v125, v125\n"
    "v_mfma_f32_32x32x16_bf16 v[80:95], v[204:207], v[196:199], v[80:95]\n"
    "ds_read_b128 v[204:207], v224 offset:2048\n"
    "v_exp_f32 v126, v126\n"
    "v_exp_f32 v127, v127\n"
    "v_exp_f32 v64, v64\n"
    "v_exp_f32 v65, v65\n"
    "v_smfmac_f32_32x32x32_bf16 v[176:191], v[212:215], v[120:127], v228\n"
    "s_waitcnt lgkmcnt(1)\n"
    "ds_read_b128 v[212:215], v226 offset:1296\n"
    "v_exp_f32 v66, v66\n"
    "v_exp_f32 v67, v67\n"
    "v_exp_f32 v68, v68\n"
    "v_exp_f32 v69, v69\n"
    "v_mfma_f32_32x32x4_2b_bf16 v[96:127], v[218:219], v[200:201], v[128:159] cbsz:1 abid:0\n"
    "v_exp_f32 v70, v70\n"
    "v_exp_f32 v71, v71\n"
    "buffer_load_dwordx4 v[16:19], v229, %[rw], %[cnt] offen offset:1024 sc1\n"
    "v_exp_f32 v72, v72\n"
    "v_exp_f32 v73, v73\n"
    "v_exp_f32 v74, v74\n"
    "v_exp_f32 v75, v75\n"
    "s_add_u32 %[cnt], %[wo], 40960\n"
    "buffer_load_dwordx4 v[20:23], v229, %[rw], %[cnt] offen offset:0 sc1\n"
    "v_exp_f32 v76, v76\n"
    "v_exp_f32 v77, v77\n"
    "v_smfmac_f32_32x32x32_bf16 v[160:175], v[208:211], v[64:71], v228\n"
    "s_waitcnt lgkmcnt(0)\n"
    "v_exp_f32 v78, v78\n"
    "v_exp_f32 v79, v79\n"
    "v_exp_f32 v80, v80\n"
    "v_exp_f32 v81, v81\n"
    "v_smfmac_f32_32x32x32_bf16 v[160:175], v[212:215], v[72:79], v228\n"
    "buffer_load_dwordx4 v[24:27], v229, %[rw], %[cnt] offen offset:256 sc1\n"
    "v_exp_f32 v82, v82\n"
    "v_exp_f32 v83, v83\n"
    "v_exp_f32 v84, v84\n"
    "v_exp_f32 v85, v85\n"
    "v_mfma_f32_32x32x16_bf16 v[96:111], v[204:207], v[192:195], v[96:111]\n"
    "buffer_load_dwordx4 v[28:31], v229, %[rw], %[cnt] offen offset:512 sc1\n"
    "v_exp_f32 v86, v86\n"
    "v_exp_f32 v87, v87\n"
    "v_exp_f32 v88, v88\n"
    "v_exp_f32 v89, v89\n"
    "v_smfmac_f32_32x32x32_bf16 v[176:191], v[208:211], v[80:87], v228\n"
    "ds_read_b128 v[208:211], v225 offset:2592\n"
    "v_exp_f32 v90, v90\n"
    "v_exp_f32 v91, v91\n"
    "v_exp_f32 v92, v92\n"
    "v_exp_f32 v93, v93\n"
    "v_mfma_f32_32x32x16_bf16 v[112:127], v[204:207], v[196:199], v[112:127]\n"
    "ds_read_b128 v[204:207], v224 offset:3072\n"
    "v_exp_f32 v94, v94\n"
    "v_exp_f32 v95, v95\n"
    "v_exp_f32 v96, v96\n"
    "v_exp_f32 v97, v97\n"
    "v_smfmac_f32_32x32x32_bf16 v[176:191], v[212:215], v[88:95], v228\n"
    "s_waitcnt lgkmcnt(1)\n"
    "ds_read_b128 v[212:215], v226 offset:2592\n"
    "v_exp_f32 v98, v98\n"
    "v_exp_f32 v99, v99\n"
    "v_exp_f32 v100, v100\n"
    "v_exp_f32 v101, v101\n"
    "v_mfma_f32_32x32x4_2b_bf16 v[64:95], v[218:219], v[200:201], v[128:159] cbsz:1 abid:1\n"
    "v_exp_f32 v102, v102\n"
    "v_exp_f32 v103, v103\n"
    "buffer_load_dwordx4 v[32:35], v229, %[rw], %[cnt] offen offset:768 sc1\n"
    "v_exp_f32 v104, v104\n"
    "v_exp_f32 v105, v105\n"
    "v_exp_f32 v106, v106\n"
    "v_exp_f32 v107, v107\n"
    "buffer_load_dwordx4 v[36:39], v229, %[rw], %[cnt] offen offset:1024 sc1\n"
    "v_exp_f32 v108, v108\n"
    "v_exp_f32 v109, v109\n"
    "v_smfmac_f32_32x32x32_bf16 v[160:175], v[208:211], v[96:103], v228\n"
    "s_waitcnt lgkmcnt(0)\n"
    "v_exp_f32 v110, v110\n"
    "v_exp_f32 v111, v111\n"
    "v_exp_f32 v112, v112\n"
    "v_exp_f32 v113, v113\n"
    "v_smfmac_f32_32x32x32_bf16 v[160:175], v[212:215], v[104:111], v228\n"
    "s_add_u32 %[cnt], %[wo], 81920\n"
    "buffer_load_dwordx4 v[40:43], v229, %[rw], %[cnt] offen offset:0 sc1\n"
    "v_exp_f32 v114, v114\n"
    "v_exp_f32 v115, v115\n"
    "v_exp_f32 v116, v116\n"
    "v_exp_f32 v117, v117\n"
    "v_mfma_f32_32x32x16_bf16 v[64:79], v[204:207], v[192:195], v[64:79]\n"
    "buffer_load_dwordx4 v[44:47], v229, %[rw], %[cnt] offen offset:256 sc1\n"
    "v_exp_f32 v118, v118\n"
    "v_exp_f32 v119, v119\n"
    "v_exp_f32 v120, v120\n"
    "v_exp_f32 v121, v121\n"
    "v_smfmac_f32_32x32x32_bf16 v[176:191], v[208:211], v[112:119], v228\n"
    "ds_read_b128 v[208:211], v225 offset:3888\n"
    "v_exp_f32 v122, v122\n"
    "v_exp_f32 v123, v123\n"
    "v_exp_f32 v124, v124\n"
    "v_exp_f32 v125, v125\n"
    "v_mfma_f32_32x32x16_bf16 v[80:95], v[204:207], v[196:199], v[80:95]\n"
    "ds_read_b128 v[204:207], v224 offset:4096\n"
    "v_exp_f32 v126, v126\n"
    "v_exp_f32 v127, v127\n"
    "v_exp_f32 v64, v64\n"
    "v_exp_f32 v65, v65\n"
    "v_smfmac_f32_32x32x32_bf16 v[176:191], v[212:215], v[120:127], v228\n"
    "s_waitcnt lgkmcnt(1)\n"
    "ds_read_b128 v[212:215], v226 offset:3888\n"
    "v_exp_f32 v66, v66\n"
    "v_exp_f32 v67, v67\n"
    "v_exp_f32 v68, v68\n"
    "v_exp_f32 v69, v69\n"
    "v_mfma_f32_32x32x4_2b_bf16 v[96:127], v[220:221], v[200:201], v[128:159] cbsz:1 abid:0\n"
    "v_exp_f32 v70, v70\n"
    "v_exp_f32 v71, v71\n"
    "buffer_load_dwordx4 v[48:51], v229, %[rw], %[cnt] offen offset:512 sc1\n"
    "v_exp_f32 v72, v72\n"
    "v_exp_f32 v73, v73\n"
    "v_exp_f32 v74, v74\n"
    "v_exp_f32 v75, v75\n"
    "buffer_load_dwordx4 v[52:55], v229, %[rw], %[cnt] offen offset:768 sc1\n"
    "v_exp_f32 v76, v76\n"
    "v_exp_f32 v77, v77\n"
    "v_smfmac_f32_32x32x32_bf16 v[160:175], v[208:211], v[64:71], v228\n"
    "s_waitcnt lgkmcnt(0)\n"
    "v_exp_f32 v78, v78\n"
    "v_exp_f32 v79, v79\n"
    "v_exp_f32 v80, v80\n"
    "v_exp_f32 v81, v81\n"
    "v_smfmac_f32_32x32x32_bf16 v[160:175], v[212:215], v[72:79], v228\n"
    "buffer_load_dwordx4 v[56:59], v229, %[rw], %[cnt] offen offset:1024 sc1\n"
    "v_exp_f32 v82, v82\n"
    "v_exp_f32 v83, v83\n"
    "v_exp_f32 v84, v84\n"
    "v_exp_f32 v85, v85\n"
    "v_mfma_f32_32x32x16_bf16 v[96:111], v[204:207], v[192:195], v[96:111]\n"
    "s_add_u32 %[cnt], %[wo], 122880\n"
    "buffer_load_dwordx4 v[60:63], v229, %[rw], %[cnt] offen offset:0 sc1\n"
    "v_exp_f32 v86, v86\n"
    "v_exp_f32 v87, v87\n"
    "v_exp_f32 v88, v88\n"
    "v_exp_f32 v89, v89\n"
    "v_smfmac_f32_32x32x32_bf16 v[176:191], v[208:211], v[80:87], v228\n"
    "ds_read_b128 v[208:211], v225 offset:5184\n"
    "v_exp_f32 v90, v90\n"
    "v_exp_f32 v91, v91\n"
    "v_exp_f32 v92, v92\n"
    "v_exp_f32 v93, v93\n"
    "v_mfma_f32_32x32x16_bf16 v[112:127], v[204:207], v[196:199], v[112:127]\n"
    "ds_read_b128 v[204:207], v224 offset:5120\n"
    "v_exp_f32 v94, v94\n"
    "v_exp_f32 v95, v95\n"
    "v_exp_f32 v96, v96\n"
    "v_exp_f32 v97, v97\n"
    "v_smfmac_f32_32x32x32_bf16 v[176:191], v[212:215], v[88:95], v228\n"
    "s_waitcnt lgkmcnt(1)\n"
    "ds_read_b128 v[212:215], v226 offset:5184\n"
    "v_exp_f32 v98, v98\n"
    "v_exp_f32 v99, v99\n"
    "v_exp_f32 v100, v100\n"
    "v_exp_f32 v101, v101\n"
    "v_mfma_f32_32x32x4_2b_bf16 v[64:95], v[220:221], v[200:201], v[128:159] cbsz:1 abid:1\n"
    "v_exp_f32 v102, v102\n"
    "v_exp_f32 v103, v103\n"
    "buffer_load_dwordx4 v[232:235], v229, %[rw], %[cnt] offen offset:256 sc1\n"
    "v_exp_f32 v104, v104\n"
    "v_exp_f32 v105, v105\n"
    "v_exp_f32 v106, v106\n"
    "v_exp_f32 v107, v107\n"
    "buffer_load_dwordx4 v[236:239], v229, %[rw], %[cnt] offen offset:512 sc1\n"
    "v_exp_f32 v108, v108\n"
    "v_exp_f32 v109, v109\n"
    "v_smfmac_f32_32x32x32_bf16 v[160:175], v[208:211], v[96:103], v228\n"
    "s_waitcnt lgkmcnt(0)\n"
    "v_exp_f32 v110, v110\n"
    "v_exp_f32 v111, v111\n"
    "v_exp_f32 v112, v112\n"
    "v_exp_f32 v113, v113\n"
    "v_smfmac_f32_32x32x32_bf16 v[160:175], v[212:215], v[104:111], v228\n"
    "buffer_load_dwordx4 v[240:243], v229, %[rw], %[cnt] offen offset:768 sc1\n"
    "v_exp_f32 v114, v114\n"
    "v_exp_f32 v115, v115\n"
    "v_exp_f32 v116, v116\n"
    "v_exp_f32 v117, v117\n"
    "v_mfma_f32_32x32x16_bf16 v[64:79], v[204:207], v[192:195], v[64:79]\n"
    "buffer_load_dwordx4 v[244:247], v229, %[rw], %[cnt] offen offset:1024 sc1\n"
    "v_exp_f32 v118, v118\n"
    "v_exp_f32 v119, v119\n"
    "v_exp_f32 v120, v120\n"
    "v_exp_f32 v121, v121\n"
    "v_smfmac_f32_32x32x32_bf16 v[176:191], v[208:211], v[112:119], v228\n"
    "ds_read_b128 v[208:211], v225 offset:6480\n"
    "v_exp_f32 v122, v122\n"
    "v_exp_f32 v123, v123\n"
    "v_exp_f32 v124, v124\n"
    "v_exp_f32 v125, v125\n"
    "v_mfma_f32_32x32x16_bf16 v[80:95], v[204:207], v[196:199], v[80:95]\n"
    "ds_read_b128 v[204:207], v224 offset:6144\n"
    "v_exp_f32 v126, v126\n"
    "v_exp_f32 v127, v127\n"
    "v_exp_f32 v64, v64\n"
    "v_exp_f32 v65, v65\n"
    "v_smfmac_f32_32x32x32_bf16 v[176:191], v[212:215], v[120:127], v228\n"
    "s_waitcnt lgkmcnt(1)\n"
    "ds_read_b128 v[212:215], v226 offset:6480\n"
    "v_exp_f32 v66, v66\n"
    "v_exp_f32 v67, v67\n"
    "v_exp_f32 v68, v68\n"
    "v_exp_f32 v69, v69\n"
    "v_mfma_f32_32x32x4_2b_bf16 v[96:127], v[222:223], v[200:201], v[128:159] cbsz:1 abid:0\n"
    "v_exp_f32 v70, v70\n"
    "v_exp_f32 v71, v71\n"
    "s_add_u32 %[cnt], %[wo], 163840\n"
    "buffer_load_dwordx4 v[248:251], v229, %[rw], %[cnt] offen offset:0 sc1\n"
    "v_exp_f32 v72, v72\n"
    "v_exp_f32 v73, v73\n"
    "v_exp_f32 v74, v74\n"
    "v_exp_f32 v75, v75\n"
    "buffer_load_dwordx4 v[252:255], v229, %[rw], %[cnt] offen offset:256 sc1\n"
    "v_exp_f32 v76, v76\n"
    "v_exp_f32 v77, v77\n"
    "v_smfmac_f32_32x32x32_bf16 v[160:175], v[208:211], v[64:71], v228\n"
    "s_waitcnt lgkmcnt(0)\n"
    "v_exp_f32 v78, v78\n"
    "v_exp_f32 v79, v79\n"
    "v_exp_f32 v80, v80\n"
    "v_exp_f32 v81, v81\n"
    "v_smfmac_f32_32x32x32_bf16 v[160:175], v[212:215], v[72:79], v228\n"
    "buffer_load_dwordx4 v[216:219], v229, %[rw], %[cnt] offen offset:512 sc1\n"
    "v_exp_f32 v82, v82\n"
    "v_exp_f32 v83, v83\n"
    "v_exp_f32 v84, v84\n"
    "v_exp_f32 v85, v85\n"
    "v_mfma_f32_32x32x16_bf16 v[96:111], v[204:207], v[192:195], v[96:111]\n"
    "v_exp_f32 v86, v86\n"
    "v_exp_f32 v87, v87\n"
    "v_exp_f32 v88, v88\n"
    "v_exp_f32 v89, v89\n"
    "v_smfmac_f32_32x32x32_bf16 v[176:191], v[208:211], v[80:87], v228\n"
    "ds_read_b128 v[208:211], v225 offset:7776\n"
    "v_exp_f32 v90, v90\n"
    "v_exp_f32 v91, v91\n"
    "v_exp_f32 v92, v92\n"
    "v_exp_f32 v93, v93\n"
    "v_mfma_f32_32x32x16_bf16 v[112:127], v[204:207], v[196:199], v[112:127]\n"
    "ds_read_b128 v[204:207], v224 offset:7168\n"
    "v_exp_f32 v94, v94\n"
    "v_exp_f32 v95, v95\n"
    "v_exp_f32 v96, v96\n"
    "v_exp_f32 v97, v97\n"
    "v_smfmac_f32_32x32x32_bf16 v[176:191], v[212:215], v[88:95], v228\n"
    "s_waitcnt lgkmcnt(1)\n"
    "ds_read_b128 v[212:215], v226 offset:7776\n"
    "v_exp_f32 v98, v98\n"
    "v_exp_f32 v99, v99\n"
    "v_exp_f32 v100, v100\n"
    "v_exp_f32 v101, v101\n"
    "v_mfma_f32_32x32x4_2b_bf16 v[64:95], v[222:223], v[200:201], v[128:159] cbsz:1 abid:1\n"
    "v_exp_f32 v102, v102\n"
    "v_exp_f32 v103, v103\n"
    "buffer_load_dwordx4 v[220:223], v229, %[rw], %[cnt] offen offset:768 sc1\n"
    "v_exp_f32 v104, v104\n"
    "v_exp_f32 v105, v105\n"
    "v_exp_f32 v106, v106\n"
    "v_exp_f32 v107, v107\n"
    "buffer_load_dwordx4 v[128:131], v229, %[rw], %[cnt] offen offset:1024 sc1\n"
    "v_exp_f32 v108, v108\n"
    "v_exp_f32 v109, v109\n"
    "v_smfmac_f32_32x32x32_bf16 v[160:175], v[208:211], v[96:103], v228\n"
    "s_waitcnt lgkmcnt(0)\n"
    "v_exp_f32 v110, v110\n"
    "v_exp_f32 v111, v111\n"
    "v_exp_f32 v112, v112\n"
    "v_exp_f32 v113, v113\n"
    "v_smfmac_f32_32x32x32_bf16 v[160:175], v[212:215], v[104:111], v228\n"
    "v_exp_f32 v114, v114\n"
    "v_exp_f32 v115, v115\n"
    "v_exp_f32 v116, v116\n"
    "v_exp_f32 v117, v117\n"
    "v_mfma_f32_32x32x16_bf16 v[64:79], v[204:207], v[192:195], v[64:79]\n"
    "v_exp_f32 v118, v118\n"
    "v_exp_f32 v119, v119\n"
    "v_exp_f32 v120, v120\n"
    "v_exp_f32 v121, v121\n"
    "v_smfmac_f32_32x32x32_bf16 v[176:191], v[208:211], v[112:119], v228\n"
    "ds_read_b128 v[208:211], v225 offset:9072\n"
    "v_exp_f32 v122, v122\n"
    "v_exp_f32 v123, v123\n"
    "v_exp_f32 v124, v124\n"
    "v_exp_f32 v125, v125\n"
    "v_mfma_f32_32x32x16_bf16 v[80:95], v[204:207], v[196:199], v[80:95]\n"
    "v_exp_f32 v126, v126\n"
    "v_exp_f32 v127, v127\n"
    "v_exp_f32 v64, v64\n"
    "v_exp_f32 v65, v65\n"
    "v_smfmac_f32_32x32x32_bf16 v[176:191], v[212:215], v[120:127], v228\n"
    "s_waitcnt lgkmcnt(0)\n"
    "ds_read_b128 v[212:215], v226 offset:9072\n"
    "v_exp_f32 v66, v66\n"
    "v_exp_f32 v67, v67\n"
    "v_exp_f32 v68, v68\n"
    "v_exp_f32 v69, v69\n"
    "v_exp_f32 v70, v70\n"
    "v_exp_f32 v71, v71\n"
    "v_exp_f32 v72, v72\n"
    "v_exp_f32 v73, v73\n"
    "v_exp_f32 v74, v74\n"
    "v_exp_f32 v75, v75\n"
    "v_exp_f32 v76, v76\n"
    "v_exp_f32 v77, v77\n"
    "v_smfmac_f32_32x32x32_bf16 v[160:175], v[208:211], v[64:71], v228\n"
    "s_waitcnt lgkmcnt(0)\n"
    "v_exp_f32 v78, v78\n"
    "v_exp_f32 v79, v79\n"
    "v_exp_f32 v80, v80\n"
    "v_exp_f32 v81, v81\n"
    "v_smfmac_f32_32x32x32_bf16 v[160:175], v[212:215], v[72:79], v228\n"
    "v_exp_f32 v82, v82\n"
    "v_exp_f32 v83, v83\n"
    "v_exp_f32 v84, v84\n"
    "v_exp_f32 v85, v85\n"
    "v_exp_f32 v86, v86\n"
    "v_exp_f32 v87, v87\n"
    "v_exp_f32 v88, v88\n"
    "v_exp_f32 v89, v89\n"
    "v_smfmac_f32_32x32x32_bf16 v[176:191], v[208:211], v[80:87], v228\n"
    "v_exp_f32 v90, v90\n"
    "v_exp_f32 v91, v91\n"
    "v_exp_f32 v92, v92\n"
    "v_exp_f32 v93, v93\n"
    "v_exp_f32 v94, v94\n"
    "v_exp_f32 v95, v95\n"
    "v_rcp_f32 v78, v172\n"
    "v_and_b32 v74, 0x3ff, v224\n"
    "v_and_b32 v76, 0x1f0, v224\n"
    "v_smfmac_f32_32x32x32_bf16 v[176:191], v[212:215], v[88:95], v228\n"
    "v_lshrrev_b32 v75, 9, v74\n"
    "v_pk_mul_f32 v[160:161], v[160:161], v[78:79] op_sel_hi:[1,0]\n"
    "v_pk_mul_f32 v[162:163], v[162:163], v[78:79] op_sel_hi:[1,0]\n"
    "v_pk_mul_f32 v[164:165], v[164:165], v[78:79] op_sel_hi:[1,0]\n"
    "v_pk_mul_f32 v[166:167], v[166:167], v[78:79] op_sel_hi:[1,0]\n"
    "v_pk_mul_f32 v[168:169], v[168:169], v[78:79] op_sel_hi:[1,0]\n"
    "v_pk_mul_f32 v[170:171], v[170:171], v[78:79] op_sel_hi:[1,0]\n"
    "v_lshl_or_b32 v75, v75, 15, v76\n"
    "s_mov_b32 %[cnt], 0\n"
    "v_cvt_pk_bf16_f32 v64, v160, v161\n"
    "v_cvt_pk_bf16_f32 v65, v162, v163\n"
    "v_cvt_pk_bf16_f32 v66, v164, v165\n"
    "v_cvt_pk_bf16_f32 v67, v166, v167\n"
    "v_cvt_pk_bf16_f32 v72, v168, v169\n"
    "v_cvt_pk_bf16_f32 v73, v170, v171\n"
    "v_max_u32 v79, v172, v188\n"
    "v_rcp_f32 v78, v188\n"
    "v_cmp_lt_u32 vcc, %[lim], v79\n"
    "s_cbranch_vccnz 8b\n"
    "buffer_store_dwordx4 v[64:67], v75, %[rw], %[so] offen sc1\n"
    "v_pk_mul_f32 v[176:177], v[176:177], v[78:79] op_sel_hi:[1,0]\n"
    "v_pk_mul_f32 v[178:179], v[178:179], v[78:79] op_sel_hi:[1,0]\n"
    "v_pk_mul_f32 v[180:181], v[180:181], v[78:79] op_sel_hi:[1,0]\n"
    "v_pk_mul_f32 v[182:183], v[182:183], v[78:79] op_sel_hi:[1,0]\n"
    "v_pk_mul_f32 v[184:185], v[184:185], v[78:79] op_sel_hi:[1,0]\n"
    "v_pk_mul_f32 v[186:187], v[186:187], v[78:79] op_sel_hi:[1,0]\n"
    "v_cvt_pk_bf16_f32 v68, v176, v177\n"
    "v_cvt_pk_bf16_f32 v69, v178, v179\n"
    "v_cvt_pk_bf16_f32 v70, v180, v181\n"
    "v_cvt_pk_bf16_f32 v71, v182, v183\n"
    "v_cvt_pk_bf16_f32 v76, v184, v185\n"
    "v_cvt_pk_bf16_f32 v77, v186, v187\n"
    "buffer_store_dwordx4 v[68:71], v75, %[rw], %[so] offen offset:512 sc1\n"
    "s_nop 0\n"
    "v_permlane32_swap_b32 v72, v76\n"
    "v_permlane32_swap_b32 v73, v77\n"
    "buffer_store_dwordx2 v[72:73], v74, %[rw], %[sr] offen sc1\n"
    "s_barrier\n"
    "9:\n"
        : ATT_INOUT, "=&{v[0:31]}"(wa), "=&{v[32:63]}"(wb), "=&{v[232:247]}"(wc), "=&{v[248:255]}"(wd), [cnt] "=&s"(cnt)
        : "{v[192:195]}"(q0), "{v[196:199]}"(q1), "{v[200:201]}"(qr), [bar] "s"(first ? 5 : -1), [rw] "s"(rw),
          [wo] "s"(swo), [so] "s"(so0), [sr] "s"(so1), [lim] "i"(first ? 0x5f7fffff : -1)
        : "v64", "v65", "v66", "v67", "v68", "v69", "v70", "v71", "v72", "v73", "v74", "v75", "v76", "v77", "v78", "v79", "v80", "v81",
          "v82", "v83", "v84", "v85", "v86", "v87", "v88", "v89", "v90", "v91", "v92", "v93", "v94", "v95", "v204", "v205", "v206",
          "v207", "v208", "v209", "v210", "v211", "v212", "v213", "v214", "v215", "vcc", "scc", "memory");
    lane = lane_id();
    return cnt;
  };
  s = score_rem(kr, qr, f32v{});
  h_part(SP);
  s = score_main(k0, q0, q1, s);
  h_part(SP + 1);
  {
    float m0 = s[0], m1 = s[16];
    tile_max(s, m0, m1);
    neg = neg_max(m0, m1);
  }
  h_part(SP + 2);
  s = score_rem(kr, qr, neg);
  h_part(SP + 3);
  s = score_main(k0, q0, q1, s);
  r0 = cat(kr, rr[0]); r1 = cat(rr[1], rr[2]);
  asm volatile("" : "+{v[96:127]}"(s), "+{v[216:219]}"(r0), "+{v[220:223]}"(r1));
  h_part(SP + 4);
  h_part(SP + 5);
  asm volatile("s_barrier" : ATT_INOUT : : "memory");
  if (__builtin_expect(run(true), 0)) {
    float m0 = -__builtin_inff(), m1 = m0;
#pragma unroll 1
    for (int t = 0; t < 64; t++) {
      s = score_main(lds128(L_K + t * 1024 + lane * 16), q0, q1, score_rem(ld64(rk, (lane & 31) * 16, krem + t * 512), qr, f32v{}));
      asm volatile("s_nop 7\n\ts_nop 7\n\ts_nop 3" : "+v"(s));
      tile_max(s, m0, m1);
    }
    r0 = cat(rem_load(ws, G, lane, 0), rem_load(ws, G, lane, 1));
    r1 = cat(rem_load(ws, G, lane, 2), rem_load(ws, G, lane, 3));
    prepare();
    neg = neg_max(m0, m1);
    o = zeros();
    s = score_main(lds128(L_K + lane * 16), q0, q1, score_rem(u32x2{r0[0], r0[1]}, qr, neg));
    asm volatile("s_nop 7\n\ts_nop 7\n\ts_nop 3" : ATT_INOUT);
    run(false);
  }

  const int mb = G.head, nb = G.qb;
  const u32 g = lane >> 4, l15 = lane & 15;
  const u32 vb = g * SLOT + (mb * 64 + w * 16 + l15) * 16, so = (mb * 64 + w * 16) * 1280 + nb * 160;
  u32x4 we = __builtin_bit_cast(u32x4, f4{neg[0], neg[1], neg[2], neg[3]});
  auto A = [&](int f) {
    if (f < 8) return u32x4{wa[4 * f], wa[4 * f + 1], wa[4 * f + 2], wa[4 * f + 3]};
    if (f < 16) return u32x4{wb[4 * f - 32], wb[4 * f - 31], wb[4 * f - 30], wb[4 * f - 29]};
    if (f < 20) return u32x4{wc[4 * f - 64], wc[4 * f - 63], wc[4 * f - 62], wc[4 * f - 61]};
    if (f < 22) return u32x4{wd[4 * f - 80], wd[4 * f - 79], wd[4 * f - 78], wd[4 * f - 77]};
    return f == 22 ? r0 : f == 23 ? r1 : we;
  };
  u32x4 B[20];
  u32 vo[4], sk[5], pw[3], pr = L_PS + w * 15360 + lane * 16, gr = L_GATE + w * 5120 + lane * 16, xa = L_X + w * 3072 + l15 * 160 + g * 8;
  u32 cn = L_CNT + lane * 4;
  asm volatile("" : "+v"(pr), "+v"(gr), "+v"(xa), "+v"(cn));
#pragma unroll
  for (int i = 0; i < 5; i++) {
    sk[i] = SZ_W + (w * 5 + i) * 4 * SLOT;
    asm volatile("" : "+s"(sk[i]));
    if (i < 4) { vo[i] = vb ^ ((i + 1) & 3) * 256; asm volatile("" : "+v"(vo[i])); }
    if (i < 3) { pw[i] = L_PS + (((i + 1) ^ w) * 15 + i * 5) * 1024 + lane * 16; asm volatile("" : "+v"(pw[i])); }
  }
  auto ldo = [&](int q) { B[q] = ld128(rw, vo[q / 5], sk[q % 5]); };
  f4 acc[2][5], part[4][5];
  u32x2 xr[5];
  u32 mx = 0;
  auto umax = [](u32 m, u32x4 v) {
    asm volatile("v_max3_u32 %0, %0, %1, %2\n\tv_max3_u32 %0, %0, %3, %4" : "+v"(m) : "v"(v[0]), "v"(v[1]), "v"(v[2]), "v"(v[3]));
    return m;
  };
  auto step = [&](int q, bool first) {
#pragma unroll
    for (int n = 0; n < 5; n++) {
      const int i = q / 5, k = q % 5;
      __builtin_amdgcn_sched_barrier(0);
      acc[i & 1][n] = mfma16(A(k * 5 + n), B[q], k ? acc[i & 1][n] : f4{});
      if (k == 0 && (i == 1 || (i && first))) *(LDSP(f4))(pw[i - 1] + n * 1024) = acc[~i & 1][n];
      if (n == 0) mx = umax(mx, B[q]);
      if (!first) continue;
      if ((n & 1) && O_DEPTH + 2 * q + n / 2 < 20) ldo(O_DEPTH + 2 * q + n / 2);
      if (q == G0 + 1 || q == G0 + 2) part[q - G0 - 1][n] = ldsf4(pr + ((q - G0 - 1) * 5 + n) * 1024);
      if (q == G1 + 1) {
        part[2][n] = ldsf4(pr + (10 + n) * 1024);
        part[3][n] = ldsf4(gr + n * 1024);
        xr[n] = *(LDSP(u32x2))(xa + n * 32);
        part[0][n] += part[1][n];
        asm volatile("" : "+v"(part[0][n]));
      }
      if (q * 5 + n >= CQ && q * 5 + n < CQ + 10 && (q * 5 + n - CQ) % 2) {
        const int m = (q * 5 + n - CQ) / 2;
        part[0][m] = f4{bflo(xr[m][0]), bfhi(xr[m][0]), bflo(xr[m][1]), bfhi(xr[m][1])} + (part[0][m] + part[2][m]) * part[3][m];
        asm volatile("" : "+v"(part[0][m]));
      }
    }
    __builtin_amdgcn_sched_barrier(0);
  };
  int spin = SPIN_MAX;
  u32 vp = (__umul24(lane >> 1, 5 * SLOT) + (lane & 1) * 8 + (SZ_W + (w * 20 + 2) * SLOT + (mb * 64 + 63) * 16)) | ((7 - lane) & OOB), pv;
  u32 vx[3];
#pragma unroll
  for (int i = 0; i < 3; i++) { vx[i] = row_chunk(i, lane); asm volatile("" : "+v"(vx[i])); }
  asm volatile("1:\n\tbuffer_load_dword %0, %1, %[ro], 0 offen sc1\n\ts_waitcnt vmcnt(0)\n\tv_cmp_eq_u32 vcc, -1, %0\n\ts_cbranch_vccz 2f\n\t"
               "s_sub_u32 %2, %2, 1\n\tv_cndmask_b32 %1, -4.0, %1, vcc\n\ts_sleep 2\n\ts_cbranch_scc0 1b\n2:"
               : "=&v"(pv), "+v"(vp), "+s"(spin), "+{v[0:31]}"(wa), "+{v[32:63]}"(wb), "+{v[232:247]}"(wc), "+{v[248:255]}"(wd),
                 "+{v[216:219]}"(r0), "+{v[220:223]}"(r1), "+{v[128:131]}"(we)
               : [ro] "s"(rw) : "vcc", "scc", "memory");
#pragma unroll
  for (int q = 0; q < O_DEPTH; q++) { ldo(q); __builtin_amdgcn_sched_barrier(0); }
  sfor<0, 20>([&](auto Q) {
    constexpr int q = Q, lo = q == 9 ? 0 : q - 4;
    step(q, true);
    if constexpr (q == 9 || q == 14 || q == 19) {
      if (__builtin_expect(__any(mx == ~0u), 0)) do {
        mx = 0;
#pragma unroll
        for (int p = lo; p <= q; p++) ldo(p);
#pragma unroll
        for (int p = lo; p <= q; p++) step(p, false);
      } while (__any(mx == ~0u) && --spin > 0);
      mx = 0;
    }
    if (q == 10) asm volatile("ds_add_u32 %0, %1" ::"v"(cn), "v"(256) : "memory");
    if (q == G0) asm volatile("1:\n\tds_read_u8 %0, %1 offset:1\n\ts_waitcnt lgkmcnt(0)\n\tv_cmp_ne_u32 vcc, 4, %0\n\ts_cbranch_vccnz 1b" : "=&v"(pv) : "v"(cn) : "vcc", "memory");
    if (q == G1) lds_bar();
  });
#pragma unroll
  for (int n = 0; n < 5; n++) {
    const f4 y = part[0][n] + acc[1][n] * part[3][n];
    *(LDSP(u32x2))(xa + n * 32) = u32x2{pk(y[0], y[1]), pk(y[2], y[3])};
  }
#pragma unroll
  for (int i = 0; i < 3; i++) B[i] = lds128(L_X + w * 3072 + i * 1024 + lane * 16);
#pragma unroll
  for (int i = 0; i < 3; i++) __builtin_amdgcn_raw_buffer_store_b128(B[i], mk(out, 2048 * 1280), vx[i], so, 0);
}

DEV void mlp_waves(const void* x, const void* b2, char* ws, u64 tag, const Geo& G, const P1Out& p1) {
  const u32 lane = lane_id();
  const int v = G.w - 4;
  {
    __builtin_amdgcn_s_setprio(0);
    f4 hv[2];
    u32x4 hq[4];
    u32x2 rq[7];
    auto rem_ld = [&](int q) {
      asm volatile("buffer_load_dwordx2 %0, %1, %2, %3 offen sc1" : "=v"(rq[q]) : "v"(lane * 16), "s"(mk(ws, WS_END)),
                   "s"(WS_QK + 80 * SLOT + G.remoff + (q * 4 + G.w) * 1024) : "memory");
    };
    sfor<0, 8>([&](auto I) {
      constexpr int i = I;
      __builtin_amdgcn_sched_barrier(0);
      if (i == XQ) {
        wait_bar<0>();
        rem_ld(0);
#pragma unroll
        for (int q = 0; q < KV_N0; q++) kv_dma(ws, G, lane, q);
        __builtin_amdgcn_sched_barrier(0);
      }
      hv[i & 1] = gelu4(p1.v[i & 3][i >> 2], p1.r[i >> 2]);
      if (i & 1) hq[i >> 1] = pair8(hv[0], hv[1]);
      keep(i & 1 ? hq[i >> 1] : __builtin_bit_cast(u32x4, hv[0]));
    });
    __builtin_amdgcn_sched_barrier(0);
#pragma unroll
    for (int i = 0; i < 2; i++) h_store(hq[i], i, ws, G.w, lane, G.rb, G.j);
    asm volatile("s_waitcnt vmcnt(2)" : "+v"(rq[0]) : : "memory");
    rem_put(rq[0], lane, v);
    *(LDSP(u32))(L_V + 1280 + lane * 1296 + v * 4) = 0x3f803f80u;
    __builtin_amdgcn_s_setprio(3);
    lds_bar();
#pragma unroll
    for (int q = 1; q < 7; q++) rem_ld(q);
#pragma unroll
    for (int i = KV_N0; i < KV_N1; i++) kv_dma(ws, G, lane, i);
#pragma unroll
    for (int i = 2; i < 4; i++) h_store(hq[i], i, ws, G.w, lane, G.rb, G.j);
    asm volatile("s_waitcnt vmcnt(0)" : "+v"(rq[1]), "+v"(rq[2]), "+v"(rq[3]), "+v"(rq[4]), "+v"(rq[5]), "+v"(rq[6]) : : "memory");
#pragma unroll
    for (int i = 1; i < 7; i++) rem_put(rq[i], lane, i * 4 + v);
#pragma unroll
    for (int i = KV_N1; i < KV_N2; i++) kv_dma(ws, G, lane, i);
    lds_bar();
    if (lane == 0 && G.j < 14) stflag(ws + F_H + ((G.rb * 2 + (v >> 1)) * 32 + G.j * 2 + (v & 1)) * 8, tag);
#pragma unroll
    for (int i = KV_N2; i < 16; i++) kv_dma(ws, G, lane, i);
    poll(ws + F_QKV + (15 * 16 + (lane & 31)) * 8, tag);
    wait_bar<0>();
  }
  const int mb = G.head, nb = G.qb;
  const u32 g = lane >> 4, l15 = lane & 15;
  f4 acc[4][5];
  u32x4 xr[3];
  u32 pa, cn;
#pragma unroll
  for (int n = 0; n < 5; n++) {
    const u32x2 b = __builtin_amdgcn_raw_buffer_load_b64(mk(b2, 1280), (nb * 80 + g * 4) * 2 + n * 32, 0, 0);
    acc[0][n] = f4{bflo(b[0]), bfhi(b[0]), bflo(b[1]), bfhi(b[1])};
  }
  poll(ws + F_H + (mb * 32 + (lane < 28 ? lane : 0)) * 8, tag);
  {
    const u32 vb = g * SLOT + (mb * 64 + v * 16 + l15) * 16;
    const u32 va = (g * 640 + nb * 80 + l15) * 16;
    u32x4 A[W2_DEPTH][5], B[W2_DEPTH][4];
    auto ld1 = [&](int i, int e) {
      if (e < 5) A[i % W2_DEPTH][e] = ld128(mk(ws + WS_W, i < 12 || v < 3 ? SZ_W : 0), va + e * 256, (v * 14 + i) * 40960);
      else B[i % W2_DEPTH][e - 5] = ld128(mk(ws + WS_H, i < 12 || v < 3 ? SZ_H : 0), vb ^ ((e - 5) * 256), (v * 14 + i) * 4 * SLOT);
    };
    auto ld = [&](auto I) {
#pragma unroll
      for (int e = 0; e < 9; e++) ld1(I, e);
    };
    auto x_load = [&] {
      const u32 l = lane_id();
#pragma unroll
      for (int n = 0; n < 3; n++) xr[n] = __builtin_amdgcn_raw_buffer_load_b128(mk(x, 2048 * 1280), row_chunk(n, l), (mb * 64 + v * 16) * 1280 + nb * 160, 0);
    };
    sfor<0, W2_DEPTH - 1>(ld);
    asm volatile("" : "+v"(acc[0][0]), "+v"(acc[0][1]), "+v"(acc[0][2]), "+v"(acc[0][3]), "+v"(acc[0][4]));
    sfor<0, 14 - W2_DEFER>([&](auto I) {
      constexpr int i = I;
      __builtin_amdgcn_sched_barrier(0);
      if constexpr (i == 0) __builtin_amdgcn_s_setprio(0);
#pragma unroll
      for (int e = 0; e < 20; e++) {
        const int m = e / 5, n = e % 5;
        acc[m][n] = mfma16(A[i % W2_DEPTH][n], B[i % W2_DEPTH][m], i || !m ? acc[m][n] : f4{});
        if (i + W2_DEPTH - 1 < 14 && e % 2 && e < 19) ld1(i + W2_DEPTH - 1, e / 2);
        if (e % 2) __builtin_amdgcn_sched_barrier(0);
      }
    });
    if constexpr (W2_DEFER == W2_DEPTH) ld(std::integral_constant<int, 13>{});
    __builtin_amdgcn_sched_barrier(0);
    x_load();
    pa = L_PM + lane_id() * 16;
    cn = L_CNT + pa / 4;
    asm volatile("s_barrier" : "+v"(pa), "+v"(cn) : : "memory");
#pragma unroll
    for (int t = 0; t < 4; t++)
#pragma unroll
      for (int e = 0; e < 5 * W2_DEFER; e++) {
        const int m = (t + 1) & 3, n = e % 5, i = (14 - W2_DEFER + e / 5) % W2_DEPTH, k = e / 3;
        __builtin_amdgcn_sched_barrier(0);
        acc[m][n] = mfma16(A[i][n], B[i][m], acc[m][n]);
        if (e % 3 != 2) continue;
        if (t && k < 5) *(LDSP(f4))(pa + ((t ^ v) * 15 + (t - 1) * 5 + k) * 1024) = acc[t][k];
        if (!t && k >= 2 && k < 5) *(LDSP(u32x4))(L_X + v * 3072 + (k - 2) * 1024 + pa) = xr[k - 2];
        if (t == 3 && k == 4) asm volatile("ds_add_u32 %0, %1" ::"v"(cn), "v"(1) : "memory");
      }
    __builtin_amdgcn_sched_barrier(0);
  }
  __builtin_amdgcn_s_setprio(3);
  {
    u32 c;
    asm volatile("1:\n\tds_read_u8 %0, %1\n\ts_waitcnt lgkmcnt(0)\n\tv_cmp_ne_u32 vcc, 8, %0\n\ts_cbranch_vccnz 1b" : "=&v"(c) : "v"(cn) : "vcc", "memory");
  }
#pragma unroll
  for (int e = 0; e < 15; e++) {
    acc[1 + e % 3][e / 3] = ldsf4(pa + (v * 15 + e % 3 * 5 + e / 3) * 1024);
    __builtin_amdgcn_sched_barrier(0);
  }
  float one = 1.f, nl2e = -1.4426950408889634f;
  asm("" : "+v"(one), "+v"(nl2e));
#pragma unroll
  for (int n = 0; n < 5; n++) {
    const f4 m = (acc[0][n] + acc[1][n]) + (acc[2][n] + acc[3][n]);
    f4 u = m * nl2e;
#pragma unroll
    for (int e = 0; e < 4; e++) u[e] = __builtin_amdgcn_exp2f(u[e]);
    u += one;
#pragma unroll
    for (int e = 0; e < 4; e++) u[e] = __builtin_amdgcn_rcpf(u[e]);
    *(LDSP(f4))(L_GATE + (v * 5 + n) * 1024 + pa) = m * u;
    __builtin_amdgcn_sched_barrier(0);
  }
  lds_bar();
}

__global__ void __launch_bounds__(512, 2)
fused_block(const void* x, const void* wqkv, const void* wo, const void* w1, const void* b1, const void* w2, const void* b2, void* out, char* ws) {
  __shared__ __attribute__((aligned(16))) char lds[163840];
  asm volatile("" ::"v"((u32)(size_t)(LDSP(char))lds) : "memory");
  const u32 lane = threadIdx.x & 63;
  const int w = __builtin_amdgcn_readfirstlane(threadIdx.x >> 6);
  const int xcd = blockIdx.x & 7, r = blockIdx.x >> 3;
  asm volatile(".Lfb_code_begin:");
  Geo G;
  G.w = w;
  const int p = (xcd >> 1) * 30 + r;
  G.rb = (xcd & 1) * 8 + (r < 30 ? p & 7 : (xcd & 6) + (r & 1));
  G.j = r < 30 ? 14 - (p >> 3) : 15;
  P1Out p1;
  const u32 rot = (xcd & 1) + (K_ROT >> (G.rb & 7) * 4 & 15);
  if (G.j != 15) phase1(x, wqkv, w1, b1, ws, G, lane, rot, p1);
  else late_geo(G);
  if (G.j >= 14) transpose_w(x, w2, wo, ws, G, lane_id(), p1);
  const u64 tag = uniform64(p1.tag[0], p1.tag[1]);
  if (w < 4) {
    __builtin_amdgcn_s_setprio(2);
    attention_waves(out, ws, G, p1);
    asm volatile("s_endpgm" : "=v"(p1.v[0][0]), "=v"(p1.v[0][1]), "=v"(p1.v[1][0]), "=v"(p1.v[1][1]), "=v"(p1.v[2][0]), "=v"(p1.v[2][1]),
                 "=v"(p1.v[3][0]), "=v"(p1.v[3][1]), "=v"(p1.r[0]), "=v"(p1.r[1]) : : "memory");
  }
  mlp_waves(x, b2, ws, tag, G, p1);
  if (blockIdx.x == 0 && w == 4) stflag(ws + F_NONCE, tag);
  asm volatile(".Lfb_code_end:");
}

extern "C" void kernel_launch(void* const* d_in, const int* in_sizes, int n_in, void* d_out, int out_size, void* d_ws, size_t ws_size,
                              hipStream_t stream) {
  fused_block<<<256, 512, 0, stream>>>(d_in[0], d_in[1], d_in[2], d_in[4], d_in[5], d_in[6], d_in[7], d_out, (char*)d_ws);
}
```
